```python
import torch

def _softmax(x, axis=-1):
    return torch.softmax(x, dim=axis)
def _gelu(x):
    return torch.nn.functional.gelu(x, approximate="tanh")
def _silu(x):
    return torch.nn.functional.silu(x)
def _sigmoid(x):
    return torch.sigmoid(x)
def _layernorm(x, axis=-1, eps=1e-5):
    m = x.mean(dim=axis, keepdim=True)
    v = x.var(dim=axis, keepdim=True, unbiased=False)
    return (x - m) / torch.sqrt(v + eps)
def _scatter_add(tgt, idx, src, axis=0):
    return tgt.clone().index_add_(axis, idx, src)
def _segment_sum(src, seg, n):
    out = torch.zeros((n,) + src.shape[1:], dtype=src.dtype)
    return out.index_add_(0, seg, src)
def _pad(x, pw):
    flat = tuple(v for pair in reversed(pw) for v in pair)
    return torch.nn.functional.pad(x, flat)

kernel_name = 'interleave_layers__linear_attn_block__swiglu_ffn__prompt'

def setup_inputs():

    _w = (lambda a: (lambda t: t.to(torch.bfloat16) if t.is_floating_point() else t)(torch.as_tensor(a)))
    def _A_setup():
        B, S, D = 1, 2048, 1024
        return {
            "x": _w((lambda *s: torch.randn(*s, dtype=torch.bfloat16) * 0.05)(B, S, D)),
            "wqkv": _w((lambda *s: torch.randn(*s, dtype=torch.bfloat16) * 0.05)(D, 3 * D)),
            "wo": _w((lambda *s: torch.randn(*s, dtype=torch.bfloat16) * 0.05)(D, D)),
        }
    _A_in = _A_setup()

    def _B_setup():
        B, S, D, F = 1, 2048, 1024, 3584
        return {
            "x": _w((lambda *s: torch.randn(*s, dtype=torch.bfloat16) * 0.05)(B, S, D)),
            "w_gate": _w((lambda *s: torch.randn(*s, dtype=torch.bfloat16) * 0.05)(D, F)),
            "w_up": _w((lambda *s: torch.randn(*s, dtype=torch.bfloat16) * 0.05)(D, F)),
            "w_down": _w((lambda *s: torch.randn(*s, dtype=torch.bfloat16) * 0.05)(F, D)),
        }
    _B_in = _B_setup()

    return {
        "A_x": _A_in["x"],
        "A_wqkv": _A_in["wqkv"],
        "A_wo": _A_in["wo"],
        "B_x": _B_in["x"],
        "B_w_gate": _B_in["w_gate"],
        "B_w_up": _B_in["w_up"],
        "B_w_down": _B_in["w_down"],
    }

def reference(A_x, A_wqkv, A_wo, B_x, B_w_gate, B_w_up, B_w_down):
    def _A_ref(x, wqkv, wo):
        B, S, D = x.shape
        H = 32; Dh = D // H
        qkv = (lambda x, *d: x.permute(*d))((x @ wqkv).reshape(B, S, 3, H, Dh), 2, 0, 3, 1, 4)
        q, k, v = qkv[0], qkv[1], qkv[2]
        phi = lambda t: torch.where(t > 0, t + 1, torch.exp(t)) + 1e-6
        pq, pk = phi(q), phi(k)
        kv = torch.einsum("bhsd,bhse->bhde", pk, v)
        z = torch.einsum("bhsd->bhd", pk)
        num = torch.einsum("bhqd,bhde->bhqe", pq, kv)
        den = torch.einsum("bhqd,bhd->bhq", pq, z)[..., None] + 1e-6
        o = num / den
        return (lambda x, *d: x.permute(*d))(o, 0, 2, 1, 3).reshape(B, S, D) @ wo

    def _B_ref(x, w_gate, w_up, w_down):
        g = _silu(x @ w_gate)
        u = x @ w_up
        return (g * u) @ w_down

    x = A_x
    x = x + _A_ref(_layernorm(x, axis=-1), A_wqkv, A_wo)
    x = x + _B_ref(_layernorm(x, axis=-1), B_w_gate, B_w_up, B_w_down)
    x = x + _A_ref(_layernorm(x, axis=-1), A_wqkv, A_wo)
    x = x + _B_ref(_layernorm(x, axis=-1), B_w_gate, B_w_up, B_w_down)
    return x
```

```cpp
#include <hip/hip_runtime.h>
#include <hip/hip_bf16.h>
#include <stdint.h>

typedef unsigned short bf16_t;
typedef short bf16x8 __attribute__((ext_vector_type(8)));
typedef float f32x4 __attribute__((ext_vector_type(4)));

static constexpr int S_ = 2048, D_ = 1024, F_ = 3584, H_ = 32, DH_ = 32;

__device__ __forceinline__ float bf2f(bf16_t v) { return __uint_as_float(((unsigned)v) << 16); }
__device__ __forceinline__ bf16_t f2bf(float f) { unsigned u = __float_as_uint(f); u += 0x7FFFu + ((u >> 16) & 1u); return (bf16_t)(u >> 16); }

__global__ void ln_kernel(const bf16_t* x, bf16_t* y) {
    __shared__ float red[8];
    int row = blockIdx.x, t = threadIdx.x;
    float v[4]; float s = 0;
    for (int i = 0; i < 4; i++) { v[i] = bf2f(x[row * D_ + t * 4 + i]); s += v[i]; }
    for (int o = 32; o >= 1; o >>= 1) s += __shfl_xor(s, o);
    if ((t & 63) == 0) red[t >> 6] = s;
    __syncthreads();
    float mean = (red[0] + red[1] + red[2] + red[3]) * (1.0f / D_);
    float q = 0;
    for (int i = 0; i < 4; i++) { v[i] -= mean; q += v[i] * v[i]; }
    for (int o = 32; o >= 1; o >>= 1) q += __shfl_xor(q, o);
    if ((t & 63) == 0) red[4 + (t >> 6)] = q;
    __syncthreads();
    float var = (red[4] + red[5] + red[6] + red[7]) * (1.0f / D_);
    float rstd = 1.0f / sqrtf(var + 1e-5f);
    for (int i = 0; i < 4; i++) y[row * D_ + t * 4 + i] = f2bf(v[i] * rstd);
}

__global__ void gemm_kernel(const bf16_t* A, const bf16_t* B, float* C, int M, int N, int K) {
    __shared__ bf16_t As[64][40];
    __shared__ bf16_t Bs[64][40];
    int bm = blockIdx.y * 64, bn = blockIdx.x * 64;
    int t = threadIdx.x, w = t >> 6, lane = t & 63;
    int wr = w >> 1, wc = w & 1;
    f32x4 acc[2][2] = {};
    for (int k0 = 0; k0 < K; k0 += 32) {
        for (int i = t; i < 64 * 32; i += 256) { int r = i >> 5, c = i & 31; As[r][c] = A[(size_t)(bm + r) * K + k0 + c]; }
        for (int i = t; i < 64 * 32; i += 256) { int k = i >> 6, n = i & 63; Bs[n][k] = B[(size_t)(k0 + k) * N + bn + n]; }
        __syncthreads();
        bf16x8 a[2], b[2];
        for (int m = 0; m < 2; m++) for (int j = 0; j < 8; j++) a[m][j] = As[wr * 32 + m * 16 + (lane & 15)][(lane >> 4) * 8 + j];
        for (int n = 0; n < 2; n++) for (int j = 0; j < 8; j++) b[n][j] = Bs[wc * 32 + n * 16 + (lane & 15)][(lane >> 4) * 8 + j];
        for (int m = 0; m < 2; m++) for (int n = 0; n < 2; n++) acc[m][n] = __builtin_amdgcn_mfma_f32_16x16x32_bf16(a[m], b[n], acc[m][n], 0, 0, 0);
        __syncthreads();
    }
    for (int m = 0; m < 2; m++) for (int n = 0; n < 2; n++) for (int j = 0; j < 4; j++)
        C[(size_t)(bm + wr * 32 + m * 16 + (lane >> 4) * 4 + j) * N + bn + wc * 32 + n * 16 + (lane & 15)] = acc[m][n][j];
}

__global__ void phi_kernel(const float* qkv, float* pqkv) {
    int i = blockIdx.x * 256 + threadIdx.x;
    int c = i % (3 * D_);
    float v = bf2f(f2bf(qkv[i]));
    if (c < 2 * D_) v = (v > 0 ? v + 1.f : __expf(v)) + 1e-6f;
    pqkv[i] = bf2f(f2bf(v));
}
__global__ void kv_kernel(const float* p, float* kv, float* z) {
    int h = blockIdx.x, t = threadIdx.x, d = t >> 5, e = t & 31;
    float s = 0, zz = 0;
    for (int m = 0; m < S_; m++) { float k = p[(size_t)m * 3 * D_ + D_ + h * 32 + d]; float v = p[(size_t)m * 3 * D_ + 2 * D_ + h * 32 + e]; s += k * v; zz += k; }
    kv[h * 1024 + t] = s; if (e == 0) z[h * 32 + d] = zz;
}
__global__ void o_kernel(const float* p, const float* kv, const float* z, bf16_t* o) {
    int i = blockIdx.x * 256 + threadIdx.x;
    int m = i / D_, c = i % D_, h = c >> 5, e = c & 31;
    float num = 0, den = 0;
    for (int d = 0; d < 32; d++) { float q = p[(size_t)m * 3 * D_ + h * 32 + d]; num += q * kv[h * 1024 + d * 32 + e]; den += q * z[h * 32 + d]; }
    o[i] = f2bf(num / (den + 1e-6f));
}
__global__ void resid_kernel(const bf16_t* x, const float* y, bf16_t* out) {
    int i = blockIdx.x * 256 + threadIdx.x;
    out[i] = f2bf(bf2f(x[i]) + y[i]);
}
__global__ void swiglu_kernel(const float* g, const float* u, bf16_t* h) {
    int i = blockIdx.x * 256 + threadIdx.x;
    float a = g[i]; float s = a / (1.f + __expf(-a));
    h[i] = f2bf(s * u[i]);
}

extern "C" void kernel_launch(void* const* d_in, const int* in_sizes, int n_in, void* d_out, int out_size, void* d_ws, size_t ws_size, hipStream_t stream) {
    const bf16_t* x0 = (const bf16_t*)d_in[0];
    const bf16_t* wqkv = (const bf16_t*)d_in[1];
    const bf16_t* wo = (const bf16_t*)d_in[2];
    const bf16_t* wg = (const bf16_t*)d_in[4];
    const bf16_t* wu = (const bf16_t*)d_in[5];
    const bf16_t* wd = (const bf16_t*)d_in[6];
    bf16_t* out = (bf16_t*)d_out;
    char* ws = (char*)d_ws;
    bf16_t* xn = (bf16_t*)ws; ws += (size_t)S_ * D_ * 2;
    bf16_t* xa = (bf16_t*)ws; ws += (size_t)S_ * D_ * 2;
    bf16_t* xb = (bf16_t*)ws; ws += (size_t)S_ * D_ * 2;
    bf16_t* ob = (bf16_t*)ws; ws += (size_t)S_ * D_ * 2;
    bf16_t* hb = (bf16_t*)ws; ws += (size_t)S_ * F_ * 2;
    float* f1 = (float*)ws; ws += (size_t)S_ * F_ * 4;
    float* f2 = (float*)ws; ws += (size_t)S_ * F_ * 4;
    float* kv = (float*)ws; ws += 32 * 1024 * 4;
    float* z = (float*)ws; ws += 1024 * 4;
    const bf16_t* x = x0;
    for (int l = 0; l < 2; l++) {
        ln_kernel<<<S_, 256, 0, stream>>>(x, xn);
        gemm_kernel<<<dim3(3 * D_ / 64, S_ / 64), 256, 0, stream>>>(xn, wqkv, f1, S_, 3 * D_, D_);
        phi_kernel<<<S_ * 3 * D_ / 256, 256, 0, stream>>>(f1, f2);
        kv_kernel<<<H_, 1024, 0, stream>>>(f2, kv, z);
        o_kernel<<<S_ * D_ / 256, 256, 0, stream>>>(f2, kv, z, ob);
        gemm_kernel<<<dim3(D_ / 64, S_ / 64), 256, 0, stream>>>(ob, wo, f1, S_, D_, D_);
        resid_kernel<<<S_ * D_ / 256, 256, 0, stream>>>(x, f1, xa);
        ln_kernel<<<S_, 256, 0, stream>>>(xa, xn);
        gemm_kernel<<<dim3(F_ / 64, S_ / 64), 256, 0, stream>>>(xn, wg, f1, S_, F_, D_);
        gemm_kernel<<<dim3(F_ / 64, S_ / 64), 256, 0, stream>>>(xn, wu, f2, S_, F_, D_);
        swiglu_kernel<<<S_ * F_ / 256, 256, 0, stream>>>(f1, f2, hb);
        gemm_kernel<<<dim3(D_ / 64, S_ / 64), 256, 0, stream>>>(hb, wd, f1, S_, D_, F_);
        bf16_t* dst = (l == 1) ? out : xb;
        resid_kernel<<<S_ * D_ / 256, 256, 0, stream>>>(xa, f1, dst);
        x = dst;
    }
}
```

```cpp
#include <hip/hip_runtime.h>
#include <hip/hip_bf16.h>
#include <stdint.h>

#define GU_STAGE 32768
#define GU_LDS 131072
#define GU_PROW(vwd, rW_, m0w_, s0_, s1_) asm volatile( \
"s_add_u32 m0, %[m0w], 16384\n" \
"s_nop 0\n" \
"buffer_load_dwordx4 v231, %[rW], %[s0] offen offset:0 lds\n" \
"buffer_load_dwordx4 v232, %[rW], %[s0] offen offset:1024 lds\n" \
"s_add_u32 m0, %[m0w], 49152\n" \
"s_nop 0\n" \
"buffer_load_dwordx4 v231, %[rW], %[s1] offen offset:0 lds\n" \
"buffer_load_dwordx4 v232, %[rW], %[s1] offen offset:1024 lds\n" \
    :: "{v231}"(vwd[0]), "{v232}"(vwd[1]), [rW] "s"(rW_), [m0w] "s"(m0w_), [s0] "s"(s0_), [s1] "s"(s1_) : "memory", "scc")
#define GU_PROX(vxd, rX_, m0x_, s0_, s1_) asm volatile( \
"s_add_u32 m0, %[m0x], 0\n" \
"s_nop 0\n" \
"buffer_load_dwordx4 v230, %[rX], %[s0] offen offset:0 sc1 lds\n" \
"buffer_load_dwordx4 v230, %[rX], %[s0] offen offset:1024 sc1 lds\n" \
"s_add_u32 m0, %[m0x], 32768\n" \
"s_nop 0\n" \
"buffer_load_dwordx4 v230, %[rX], %[s1] offen offset:0 sc1 lds\n" \
"buffer_load_dwordx4 v230, %[rX], %[s1] offen offset:1024 sc1 lds\n" \
    :: "{v230}"(vxd[0]), [rX] "s"(rX_), [m0x] "s"(m0x_), [s0] "s"(s0_), [s1] "s"(s1_) : "memory", "scc")
#define GU_MAIN(acc, vxd, vwd, vxr, vwr, rX_, rW_, sX_, sW_, m0x_, m0w_, cnt_, u_, rot_, tp_) asm volatile( \
"s_waitcnt lgkmcnt(0)\n" \
"s_waitcnt vmcnt(2)\n" \
"s_barrier\n" \
"ds_read_b64_tr_b16 v[192:193], v226 offset:16384\n" \
"ds_read_b64_tr_b16 v[194:195], v226 offset:16896\n" \
"s_add_u32 m0, %[m0x], 65536\n" \
"s_nop 0\n" \
"buffer_load_dwordx4 v230, %[rX], %[sX] offen offset:0 sc1 lds\n" \
"ds_read_b128 v[128:131], v224 offset:0\n" \
"ds_read_b128 v[132:135], v224 offset:256\n" \
"buffer_load_dwordx4 v230, %[rX], %[sX] offen offset:1024 sc1 lds\n" \
"ds_read_b128 v[136:139], v224 offset:2048\n" \
"ds_read_b128 v[140:143], v224 offset:2304\n" \
"ds_read_b128 v[144:147], v224 offset:4096\n" \
"s_add_u32 m0, %[m0w], 81920\n" \
"s_nop 0\n" \
"buffer_load_dwordx4 v231, %[rW], %[sW] offen offset:0 lds\n" \
"ds_read_b128 v[148:151], v224 offset:4352\n" \
"ds_read_b128 v[152:155], v224 offset:6144\n" \
"buffer_load_dwordx4 v232, %[rW], %[sW] offen offset:1024 lds\n" \
"s_add_u32 %[u], %[u], 1\n" \
"s_add_u32 %[tp], %[u], %[rot]\n" \
"s_and_b32 %[tp], %[tp], 7\n" \
"s_and_b32 %[sX], %[u], 2147483640\n" \
"s_or_b32 %[tp], %[tp], %[sX]\n" \
"s_mul_i32 %[sX], %[tp], 2048\n" \
"s_mul_i32 %[sW], %[tp], 229376\n" \
"ds_read_b128 v[156:159], v224 offset:6400\n" \
"ds_read_b64_tr_b16 v[196:197], v228 offset:16384\n" \
"ds_read_b64_tr_b16 v[198:199], v228 offset:16896\n" \
"s_add_u32 m0, %[m0x], 98304\n" \
"s_nop 0\n" \
"buffer_load_dwordx4 v230, %[rX], %[sX] offen offset:0 sc1 lds\n" \
"ds_read_b64_tr_b16 v[200:201], v226 offset:24576\n" \
"ds_read_b64_tr_b16 v[202:203], v226 offset:25088\n" \
"buffer_load_dwordx4 v230, %[rX], %[sX] offen offset:1024 sc1 lds\n" \
"ds_read_b64_tr_b16 v[204:205], v228 offset:24576\n" \
"ds_read_b64_tr_b16 v[206:207], v228 offset:25088\n" \
"s_add_u32 m0, %[m0w], 114688\n" \
"s_nop 0\n" \
"buffer_load_dwordx4 v231, %[rW], %[sW] offen offset:0 lds\n" \
"buffer_load_dwordx4 v232, %[rW], %[sW] offen offset:1024 lds\n" \
"s_add_u32 %[u], %[u], 1\n" \
"s_add_u32 %[tp], %[u], %[rot]\n" \
"s_and_b32 %[tp], %[tp], 7\n" \
"s_and_b32 %[sX], %[u], 2147483640\n" \
"s_or_b32 %[tp], %[tp], %[sX]\n" \
"s_mul_i32 %[sX], %[tp], 2048\n" \
"s_mul_i32 %[sW], %[tp], 229376\n" \
"s_waitcnt lgkmcnt(13)\n" \
"v_mfma_f32_16x16x32_bf16 v[0:3], v[192:195], v[128:131], 0\n" \
"s_waitcnt lgkmcnt(12)\n" \
"v_mfma_f32_16x16x32_bf16 v[4:7], v[192:195], v[132:135], 0\n" \
"s_waitcnt lgkmcnt(11)\n" \
"v_mfma_f32_16x16x32_bf16 v[8:11], v[192:195], v[136:139], 0\n" \
"s_waitcnt lgkmcnt(10)\n" \
"v_mfma_f32_16x16x32_bf16 v[12:15], v[192:195], v[140:143], 0\n" \
"s_waitcnt vmcnt(8) lgkmcnt(0)\n" \
"s_barrier\n" \
"v_mfma_f32_16x16x32_bf16 v[16:19], v[192:195], v[144:147], 0\n" \
"ds_read_b64_tr_b16 v[208:209], v226 offset:49152\n" \
"v_mfma_f32_16x16x32_bf16 v[20:23], v[192:195], v[148:151], 0\n" \
"ds_read_b64_tr_b16 v[210:211], v226 offset:49664\n" \
"v_mfma_f32_16x16x32_bf16 v[24:27], v[192:195], v[152:155], 0\n" \
"ds_read_b128 v[160:163], v224 offset:32768\n" \
"v_mfma_f32_16x16x32_bf16 v[28:31], v[192:195], v[156:159], 0\n" \
"s_add_u32 m0, %[m0x], 0\n" \
"s_nop 0\n" \
"buffer_load_dwordx4 v230, %[rX], %[sX] offen offset:0 sc1 lds\n" \
"v_mfma_f32_16x16x32_bf16 v[32:35], v[196:199], v[128:131], 0\n" \
"ds_read_b128 v[164:167], v224 offset:33024\n" \
"v_mfma_f32_16x16x32_bf16 v[36:39], v[196:199], v[132:135], 0\n" \
"ds_read_b128 v[168:171], v224 offset:34816\n" \
"v_mfma_f32_16x16x32_bf16 v[40:43], v[196:199], v[136:139], 0\n" \
"v_mfma_f32_16x16x32_bf16 v[44:47], v[196:199], v[140:143], 0\n" \
"ds_read_b128 v[172:175], v224 offset:35072\n" \
"v_mfma_f32_16x16x32_bf16 v[48:51], v[196:199], v[144:147], 0\n" \
"ds_read_b128 v[176:179], v224 offset:36864\n" \
"v_mfma_f32_16x16x32_bf16 v[52:55], v[196:199], v[148:151], 0\n" \
"ds_read_b128 v[180:183], v224 offset:37120\n" \
"v_mfma_f32_16x16x32_bf16 v[56:59], v[196:199], v[152:155], 0\n" \
"buffer_load_dwordx4 v230, %[rX], %[sX] offen offset:1024 sc1 lds\n" \
"v_mfma_f32_16x16x32_bf16 v[60:63], v[196:199], v[156:159], 0\n" \
"ds_read_b128 v[184:187], v224 offset:38912\n" \
"v_mfma_f32_16x16x32_bf16 v[64:67], v[200:203], v[128:131], 0\n" \
"ds_read_b128 v[188:191], v224 offset:39168\n" \
"v_mfma_f32_16x16x32_bf16 v[68:71], v[200:203], v[132:135], 0\n" \
"v_mfma_f32_16x16x32_bf16 v[72:75], v[200:203], v[136:139], 0\n" \
"ds_read_b64_tr_b16 v[212:213], v228 offset:49152\n" \
"v_mfma_f32_16x16x32_bf16 v[76:79], v[200:203], v[140:143], 0\n" \
"ds_read_b64_tr_b16 v[214:215], v228 offset:49664\n" \
"v_mfma_f32_16x16x32_bf16 v[80:83], v[200:203], v[144:147], 0\n" \
"ds_read_b64_tr_b16 v[216:217], v226 offset:57344\n" \
"v_mfma_f32_16x16x32_bf16 v[84:87], v[200:203], v[148:151], 0\n" \
"s_add_u32 m0, %[m0w], 16384\n" \
"s_nop 0\n" \
"buffer_load_dwordx4 v231, %[rW], %[sW] offen offset:0 lds\n" \
"v_mfma_f32_16x16x32_bf16 v[88:91], v[200:203], v[152:155], 0\n" \
"ds_read_b64_tr_b16 v[218:219], v226 offset:57856\n" \
"v_mfma_f32_16x16x32_bf16 v[92:95], v[200:203], v[156:159], 0\n" \
"ds_read_b64_tr_b16 v[220:221], v228 offset:57344\n" \
"v_mfma_f32_16x16x32_bf16 v[96:99], v[204:207], v[128:131], 0\n" \
"v_mfma_f32_16x16x32_bf16 v[100:103], v[204:207], v[132:135], 0\n" \
"ds_read_b64_tr_b16 v[222:223], v228 offset:57856\n" \
"v_mfma_f32_16x16x32_bf16 v[104:107], v[204:207], v[136:139], 0\n" \
"v_mfma_f32_16x16x32_bf16 v[108:111], v[204:207], v[140:143], 0\n" \
"v_mfma_f32_16x16x32_bf16 v[112:115], v[204:207], v[144:147], 0\n" \
"buffer_load_dwordx4 v232, %[rW], %[sW] offen offset:1024 lds\n" \
"s_add_u32 %[u], %[u], 1\n" \
"s_add_u32 %[tp], %[u], %[rot]\n" \
"s_and_b32 %[tp], %[tp], 7\n" \
"s_and_b32 %[sX], %[u], 2147483640\n" \
"s_or_b32 %[tp], %[tp], %[sX]\n" \
"s_mul_i32 %[sX], %[tp], 2048\n" \
"s_mul_i32 %[sW], %[tp], 229376\n" \
"v_mfma_f32_16x16x32_bf16 v[116:119], v[204:207], v[148:151], 0\n" \
"v_mfma_f32_16x16x32_bf16 v[120:123], v[204:207], v[152:155], 0\n" \
"v_mfma_f32_16x16x32_bf16 v[124:127], v[204:207], v[156:159], 0\n" \
"s_waitcnt lgkmcnt(13)\n" \
"v_mfma_f32_16x16x32_bf16 v[0:3], v[208:211], v[160:163], v[0:3]\n" \
"s_waitcnt lgkmcnt(12)\n" \
"v_mfma_f32_16x16x32_bf16 v[4:7], v[208:211], v[164:167], v[4:7]\n" \
"s_waitcnt lgkmcnt(11)\n" \
"v_mfma_f32_16x16x32_bf16 v[8:11], v[208:211], v[168:171], v[8:11]\n" \
"s_waitcnt lgkmcnt(10)\n" \
"v_mfma_f32_16x16x32_bf16 v[12:15], v[208:211], v[172:175], v[12:15]\n" \
"s_waitcnt vmcnt(8) lgkmcnt(0)\n" \
"s_barrier\n" \
"s_mov_b32 %[cnt], 6\n" \
"LOOP_%=:\n" \
"v_mfma_f32_16x16x32_bf16 v[16:19], v[208:211], v[176:179], v[16:19]\n" \
"ds_read_b64_tr_b16 v[192:193], v227 offset:16384\n" \
"v_mfma_f32_16x16x32_bf16 v[20:23], v[208:211], v[180:183], v[20:23]\n" \
"ds_read_b64_tr_b16 v[194:195], v227 offset:16896\n" \
"v_mfma_f32_16x16x32_bf16 v[24:27], v[208:211], v[184:187], v[24:27]\n" \
"ds_read_b128 v[128:131], v225 offset:0\n" \
"v_mfma_f32_16x16x32_bf16 v[28:31], v[208:211], v[188:191], v[28:31]\n" \
"s_add_u32 m0, %[m0x], 32768\n" \
"s_nop 0\n" \
"buffer_load_dwordx4 v230, %[rX], %[sX] offen offset:0 sc1 lds\n" \
"v_mfma_f32_16x16x32_bf16 v[32:35], v[212:215], v[160:163], v[32:35]\n" \
"ds_read_b128 v[132:135], v225 offset:256\n" \
"v_mfma_f32_16x16x32_bf16 v[36:39], v[212:215], v[164:167], v[36:39]\n" \
"ds_read_b128 v[136:139], v225 offset:2048\n" \
"v_mfma_f32_16x16x32_bf16 v[40:43], v[212:215], v[168:171], v[40:43]\n" \
"v_mfma_f32_16x16x32_bf16 v[44:47], v[212:215], v[172:175], v[44:47]\n" \
"ds_read_b128 v[140:143], v225 offset:2304\n" \
"v_mfma_f32_16x16x32_bf16 v[48:51], v[212:215], v[176:179], v[48:51]\n" \
"ds_read_b128 v[144:147], v225 offset:4096\n" \
"v_mfma_f32_16x16x32_bf16 v[52:55], v[212:215], v[180:183], v[52:55]\n" \
"ds_read_b128 v[148:151], v225 offset:4352\n" \
"v_mfma_f32_16x16x32_bf16 v[56:59], v[212:215], v[184:187], v[56:59]\n" \
"buffer_load_dwordx4 v230, %[rX], %[sX] offen offset:1024 sc1 lds\n" \
"v_mfma_f32_16x16x32_bf16 v[60:63], v[212:215], v[188:191], v[60:63]\n" \
"ds_read_b128 v[152:155], v225 offset:6144\n" \
"v_mfma_f32_16x16x32_bf16 v[64:67], v[216:219], v[160:163], v[64:67]\n" \
"ds_read_b128 v[156:159], v225 offset:6400\n" \
"v_mfma_f32_16x16x32_bf16 v[68:71], v[216:219], v[164:167], v[68:71]\n" \
"v_mfma_f32_16x16x32_bf16 v[72:75], v[216:219], v[168:171], v[72:75]\n" \
"ds_read_b64_tr_b16 v[196:197], v229 offset:16384\n" \
"v_mfma_f32_16x16x32_bf16 v[76:79], v[216:219], v[172:175], v[76:79]\n" \
"ds_read_b64_tr_b16 v[198:199], v229 offset:16896\n" \
"v_mfma_f32_16x16x32_bf16 v[80:83], v[216:219], v[176:179], v[80:83]\n" \
"ds_read_b64_tr_b16 v[200:201], v227 offset:24576\n" \
"v_mfma_f32_16x16x32_bf16 v[84:87], v[216:219], v[180:183], v[84:87]\n" \
"s_add_u32 m0, %[m0w], 49152\n" \
"s_nop 0\n" \
"buffer_load_dwordx4 v231, %[rW], %[sW] offen offset:0 lds\n" \
"v_mfma_f32_16x16x32_bf16 v[88:91], v[216:219], v[184:187], v[88:91]\n" \
"ds_read_b64_tr_b16 v[202:203], v227 offset:25088\n" \
"v_mfma_f32_16x16x32_bf16 v[92:95], v[216:219], v[188:191], v[92:95]\n" \
"ds_read_b64_tr_b16 v[204:205], v229 offset:24576\n" \
"v_mfma_f32_16x16x32_bf16 v[96:99], v[220:223], v[160:163], v[96:99]\n" \
"v_mfma_f32_16x16x32_bf16 v[100:103], v[220:223], v[164:167], v[100:103]\n" \
"ds_read_b64_tr_b16 v[206:207], v229 offset:25088\n" \
"v_mfma_f32_16x16x32_bf16 v[104:107], v[220:223], v[168:171], v[104:107]\n" \
"v_mfma_f32_16x16x32_bf16 v[108:111], v[220:223], v[172:175], v[108:111]\n" \
"v_mfma_f32_16x16x32_bf16 v[112:115], v[220:223], v[176:179], v[112:115]\n" \
"buffer_load_dwordx4 v232, %[rW], %[sW] offen offset:1024 lds\n" \
"s_add_u32 %[u], %[u], 1\n" \
"s_add_u32 %[tp], %[u], %[rot]\n" \
"s_and_b32 %[tp], %[tp], 7\n" \
"s_and_b32 %[sX], %[u], 2147483640\n" \
"s_or_b32 %[tp], %[tp], %[sX]\n" \
"s_mul_i32 %[sX], %[tp], 2048\n" \
"s_mul_i32 %[sW], %[tp], 229376\n" \
"v_mfma_f32_16x16x32_bf16 v[116:119], v[220:223], v[180:183], v[116:119]\n" \
"v_mfma_f32_16x16x32_bf16 v[120:123], v[220:223], v[184:187], v[120:123]\n" \
"v_mfma_f32_16x16x32_bf16 v[124:127], v[220:223], v[188:191], v[124:127]\n" \
"s_waitcnt lgkmcnt(13)\n" \
"v_mfma_f32_16x16x32_bf16 v[0:3], v[192:195], v[128:131], v[0:3]\n" \
"s_waitcnt lgkmcnt(12)\n" \
"v_mfma_f32_16x16x32_bf16 v[4:7], v[192:195], v[132:135], v[4:7]\n" \
"s_waitcnt lgkmcnt(11)\n" \
"v_mfma_f32_16x16x32_bf16 v[8:11], v[192:195], v[136:139], v[8:11]\n" \
"s_waitcnt lgkmcnt(10)\n" \
"v_mfma_f32_16x16x32_bf16 v[12:15], v[192:195], v[140:143], v[12:15]\n" \
"s_waitcnt vmcnt(8) lgkmcnt(0)\n" \
"s_barrier\n" \
"v_mfma_f32_16x16x32_bf16 v[16:19], v[192:195], v[144:147], v[16:19]\n" \
"ds_read_b64_tr_b16 v[208:209], v227 offset:49152\n" \
"v_mfma_f32_16x16x32_bf16 v[20:23], v[192:195], v[148:151], v[20:23]\n" \
"ds_read_b64_tr_b16 v[210:211], v227 offset:49664\n" \
"v_mfma_f32_16x16x32_bf16 v[24:27], v[192:195], v[152:155], v[24:27]\n" \
"ds_read_b128 v[160:163], v225 offset:32768\n" \
"v_mfma_f32_16x16x32_bf16 v[28:31], v[192:195], v[156:159], v[28:31]\n" \
"s_add_u32 m0, %[m0x], 65536\n" \
"s_nop 0\n" \
"buffer_load_dwordx4 v230, %[rX], %[sX] offen offset:0 sc1 lds\n" \
"v_mfma_f32_16x16x32_bf16 v[32:35], v[196:199], v[128:131], v[32:35]\n" \
"ds_read_b128 v[164:167], v225 offset:33024\n" \
"v_mfma_f32_16x16x32_bf16 v[36:39], v[196:199], v[132:135], v[36:39]\n" \
"ds_read_b128 v[168:171], v225 offset:34816\n" \
"v_mfma_f32_16x16x32_bf16 v[40:43], v[196:199], v[136:139], v[40:43]\n" \
"v_mfma_f32_16x16x32_bf16 v[44:47], v[196:199], v[140:143], v[44:47]\n" \
"ds_read_b128 v[172:175], v225 offset:35072\n" \
"v_mfma_f32_16x16x32_bf16 v[48:51], v[196:199], v[144:147], v[48:51]\n" \
"ds_read_b128 v[176:179], v225 offset:36864\n" \
"v_mfma_f32_16x16x32_bf16 v[52:55], v[196:199], v[148:151], v[52:55]\n" \
"ds_read_b128 v[180:183], v225 offset:37120\n" \
"v_mfma_f32_16x16x32_bf16 v[56:59], v[196:199], v[152:155], v[56:59]\n" \
"buffer_load_dwordx4 v230, %[rX], %[sX] offen offset:1024 sc1 lds\n" \
"v_mfma_f32_16x16x32_bf16 v[60:63], v[196:199], v[156:159], v[60:63]\n" \
"ds_read_b128 v[184:187], v225 offset:38912\n" \
"v_mfma_f32_16x16x32_bf16 v[64:67], v[200:203], v[128:131], v[64:67]\n" \
"ds_read_b128 v[188:191], v225 offset:39168\n" \
"v_mfma_f32_16x16x32_bf16 v[68:71], v[200:203], v[132:135], v[68:71]\n" \
"v_mfma_f32_16x16x32_bf16 v[72:75], v[200:203], v[136:139], v[72:75]\n" \
"ds_read_b64_tr_b16 v[212:213], v229 offset:49152\n" \
"v_mfma_f32_16x16x32_bf16 v[76:79], v[200:203], v[140:143], v[76:79]\n" \
"ds_read_b64_tr_b16 v[214:215], v229 offset:49664\n" \
"v_mfma_f32_16x16x32_bf16 v[80:83], v[200:203], v[144:147], v[80:83]\n" \
"ds_read_b64_tr_b16 v[216:217], v227 offset:57344\n" \
"v_mfma_f32_16x16x32_bf16 v[84:87], v[200:203], v[148:151], v[84:87]\n" \
"s_add_u32 m0, %[m0w], 81920\n" \
"s_nop 0\n" \
"buffer_load_dwordx4 v231, %[rW], %[sW] offen offset:0 lds\n" \
"v_mfma_f32_16x16x32_bf16 v[88:91], v[200:203], v[152:155], v[88:91]\n" \
"ds_read_b64_tr_b16 v[218:219], v227 offset:57856\n" \
"v_mfma_f32_16x16x32_bf16 v[92:95], v[200:203], v[156:159], v[92:95]\n" \
"ds_read_b64_tr_b16 v[220:221], v229 offset:57344\n" \
"v_mfma_f32_16x16x32_bf16 v[96:99], v[204:207], v[128:131], v[96:99]\n" \
"v_mfma_f32_16x16x32_bf16 v[100:103], v[204:207], v[132:135], v[100:103]\n" \
"ds_read_b64_tr_b16 v[222:223], v229 offset:57856\n" \
"v_mfma_f32_16x16x32_bf16 v[104:107], v[204:207], v[136:139], v[104:107]\n" \
"v_mfma_f32_16x16x32_bf16 v[108:111], v[204:207], v[140:143], v[108:111]\n" \
"v_mfma_f32_16x16x32_bf16 v[112:115], v[204:207], v[144:147], v[112:115]\n" \
"buffer_load_dwordx4 v232, %[rW], %[sW] offen offset:1024 lds\n" \
"s_add_u32 %[u], %[u], 1\n" \
"s_add_u32 %[tp], %[u], %[rot]\n" \
"s_and_b32 %[tp], %[tp], 7\n" \
"s_and_b32 %[sX], %[u], 2147483640\n" \
"s_or_b32 %[tp], %[tp], %[sX]\n" \
"s_mul_i32 %[sX], %[tp], 2048\n" \
"s_mul_i32 %[sW], %[tp], 229376\n" \
"v_mfma_f32_16x16x32_bf16 v[116:119], v[204:207], v[148:151], v[116:119]\n" \
"v_mfma_f32_16x16x32_bf16 v[120:123], v[204:207], v[152:155], v[120:123]\n" \
"v_mfma_f32_16x16x32_bf16 v[124:127], v[204:207], v[156:159], v[124:127]\n" \
"s_waitcnt lgkmcnt(13)\n" \
"v_mfma_f32_16x16x32_bf16 v[0:3], v[208:211], v[160:163], v[0:3]\n" \
"s_waitcnt lgkmcnt(12)\n" \
"v_mfma_f32_16x16x32_bf16 v[4:7], v[208:211], v[164:167], v[4:7]\n" \
"s_waitcnt lgkmcnt(11)\n" \
"v_mfma_f32_16x16x32_bf16 v[8:11], v[208:211], v[168:171], v[8:11]\n" \
"s_waitcnt lgkmcnt(10)\n" \
"v_mfma_f32_16x16x32_bf16 v[12:15], v[208:211], v[172:175], v[12:15]\n" \
"s_waitcnt vmcnt(8) lgkmcnt(0)\n" \
"s_barrier\n" \
"v_mfma_f32_16x16x32_bf16 v[16:19], v[208:211], v[176:179], v[16:19]\n" \
"ds_read_b64_tr_b16 v[192:193], v226 offset:16384\n" \
"v_mfma_f32_16x16x32_bf16 v[20:23], v[208:211], v[180:183], v[20:23]\n" \
"ds_read_b64_tr_b16 v[194:195], v226 offset:16896\n" \
"v_mfma_f32_16x16x32_bf16 v[24:27], v[208:211], v[184:187], v[24:27]\n" \
"ds_read_b128 v[128:131], v224 offset:0\n" \
"v_mfma_f32_16x16x32_bf16 v[28:31], v[208:211], v[188:191], v[28:31]\n" \
"s_add_u32 m0, %[m0x], 98304\n" \
"s_nop 0\n" \
"buffer_load_dwordx4 v230, %[rX], %[sX] offen offset:0 sc1 lds\n" \
"v_mfma_f32_16x16x32_bf16 v[32:35], v[212:215], v[160:163], v[32:35]\n" \
"ds_read_b128 v[132:135], v224 offset:256\n" \
"v_mfma_f32_16x16x32_bf16 v[36:39], v[212:215], v[164:167], v[36:39]\n" \
"ds_read_b128 v[136:139], v224 offset:2048\n" \
"v_mfma_f32_16x16x32_bf16 v[40:43], v[212:215], v[168:171], v[40:43]\n" \
"v_mfma_f32_16x16x32_bf16 v[44:47], v[212:215], v[172:175], v[44:47]\n" \
"ds_read_b128 v[140:143], v224 offset:2304\n" \
"v_mfma_f32_16x16x32_bf16 v[48:51], v[212:215], v[176:179], v[48:51]\n" \
"ds_read_b128 v[144:147], v224 offset:4096\n" \
"v_mfma_f32_16x16x32_bf16 v[52:55], v[212:215], v[180:183], v[52:55]\n" \
"ds_read_b128 v[148:151], v224 offset:4352\n" \
"v_mfma_f32_16x16x32_bf16 v[56:59], v[212:215], v[184:187], v[56:59]\n" \
"buffer_load_dwordx4 v230, %[rX], %[sX] offen offset:1024 sc1 lds\n" \
"v_mfma_f32_16x16x32_bf16 v[60:63], v[212:215], v[188:191], v[60:63]\n" \
"ds_read_b128 v[152:155], v224 offset:6144\n" \
"v_mfma_f32_16x16x32_bf16 v[64:67], v[216:219], v[160:163], v[64:67]\n" \
"ds_read_b128 v[156:159], v224 offset:6400\n" \
"v_mfma_f32_16x16x32_bf16 v[68:71], v[216:219], v[164:167], v[68:71]\n" \
"v_mfma_f32_16x16x32_bf16 v[72:75], v[216:219], v[168:171], v[72:75]\n" \
"ds_read_b64_tr_b16 v[196:197], v228 offset:16384\n" \
"v_mfma_f32_16x16x32_bf16 v[76:79], v[216:219], v[172:175], v[76:79]\n" \
"ds_read_b64_tr_b16 v[198:199], v228 offset:16896\n" \
"v_mfma_f32_16x16x32_bf16 v[80:83], v[216:219], v[176:179], v[80:83]\n" \
"ds_read_b64_tr_b16 v[200:201], v226 offset:24576\n" \
"v_mfma_f32_16x16x32_bf16 v[84:87], v[216:219], v[180:183], v[84:87]\n" \
"s_add_u32 m0, %[m0w], 114688\n" \
"s_nop 0\n" \
"buffer_load_dwordx4 v231, %[rW], %[sW] offen offset:0 lds\n" \
"v_mfma_f32_16x16x32_bf16 v[88:91], v[216:219], v[184:187], v[88:91]\n" \
"ds_read_b64_tr_b16 v[202:203], v226 offset:25088\n" \
"v_mfma_f32_16x16x32_bf16 v[92:95], v[216:219], v[188:191], v[92:95]\n" \
"ds_read_b64_tr_b16 v[204:205], v228 offset:24576\n" \
"v_mfma_f32_16x16x32_bf16 v[96:99], v[220:223], v[160:163], v[96:99]\n" \
"v_mfma_f32_16x16x32_bf16 v[100:103], v[220:223], v[164:167], v[100:103]\n" \
"ds_read_b64_tr_b16 v[206:207], v228 offset:25088\n" \
"v_mfma_f32_16x16x32_bf16 v[104:107], v[220:223], v[168:171], v[104:107]\n" \
"v_mfma_f32_16x16x32_bf16 v[108:111], v[220:223], v[172:175], v[108:111]\n" \
"v_mfma_f32_16x16x32_bf16 v[112:115], v[220:223], v[176:179], v[112:115]\n" \
"buffer_load_dwordx4 v232, %[rW], %[sW] offen offset:1024 lds\n" \
"s_add_u32 %[u], %[u], 1\n" \
"s_add_u32 %[tp], %[u], %[rot]\n" \
"s_and_b32 %[tp], %[tp], 7\n" \
"s_and_b32 %[sX], %[u], 2147483640\n" \
"s_or_b32 %[tp], %[tp], %[sX]\n" \
"s_mul_i32 %[sX], %[tp], 2048\n" \
"s_mul_i32 %[sW], %[tp], 229376\n" \
"v_mfma_f32_16x16x32_bf16 v[116:119], v[220:223], v[180:183], v[116:119]\n" \
"v_mfma_f32_16x16x32_bf16 v[120:123], v[220:223], v[184:187], v[120:123]\n" \
"v_mfma_f32_16x16x32_bf16 v[124:127], v[220:223], v[188:191], v[124:127]\n" \
"s_waitcnt lgkmcnt(13)\n" \
"v_mfma_f32_16x16x32_bf16 v[0:3], v[192:195], v[128:131], v[0:3]\n" \
"s_waitcnt lgkmcnt(12)\n" \
"v_mfma_f32_16x16x32_bf16 v[4:7], v[192:195], v[132:135], v[4:7]\n" \
"s_waitcnt lgkmcnt(11)\n" \
"v_mfma_f32_16x16x32_bf16 v[8:11], v[192:195], v[136:139], v[8:11]\n" \
"s_waitcnt lgkmcnt(10)\n" \
"v_mfma_f32_16x16x32_bf16 v[12:15], v[192:195], v[140:143], v[12:15]\n" \
"s_waitcnt vmcnt(8) lgkmcnt(0)\n" \
"s_barrier\n" \
"v_mfma_f32_16x16x32_bf16 v[16:19], v[192:195], v[144:147], v[16:19]\n" \
"ds_read_b64_tr_b16 v[208:209], v226 offset:49152\n" \
"v_mfma_f32_16x16x32_bf16 v[20:23], v[192:195], v[148:151], v[20:23]\n" \
"ds_read_b64_tr_b16 v[210:211], v226 offset:49664\n" \
"v_mfma_f32_16x16x32_bf16 v[24:27], v[192:195], v[152:155], v[24:27]\n" \
"ds_read_b128 v[160:163], v224 offset:32768\n" \
"v_mfma_f32_16x16x32_bf16 v[28:31], v[192:195], v[156:159], v[28:31]\n" \
"s_add_u32 m0, %[m0x], 0\n" \
"s_nop 0\n" \
"buffer_load_dwordx4 v230, %[rX], %[sX] offen offset:0 sc1 lds\n" \
"v_mfma_f32_16x16x32_bf16 v[32:35], v[196:199], v[128:131], v[32:35]\n" \
"ds_read_b128 v[164:167], v224 offset:33024\n" \
"v_mfma_f32_16x16x32_bf16 v[36:39], v[196:199], v[132:135], v[36:39]\n" \
"ds_read_b128 v[168:171], v224 offset:34816\n" \
"v_mfma_f32_16x16x32_bf16 v[40:43], v[196:199], v[136:139], v[40:43]\n" \
"v_mfma_f32_16x16x32_bf16 v[44:47], v[196:199], v[140:143], v[44:47]\n" \
"ds_read_b128 v[172:175], v224 offset:35072\n" \
"v_mfma_f32_16x16x32_bf16 v[48:51], v[196:199], v[144:147], v[48:51]\n" \
"ds_read_b128 v[176:179], v224 offset:36864\n" \
"v_mfma_f32_16x16x32_bf16 v[52:55], v[196:199], v[148:151], v[52:55]\n" \
"ds_read_b128 v[180:183], v224 offset:37120\n" \
"v_mfma_f32_16x16x32_bf16 v[56:59], v[196:199], v[152:155], v[56:59]\n" \
"buffer_load_dwordx4 v230, %[rX], %[sX] offen offset:1024 sc1 lds\n" \
"v_mfma_f32_16x16x32_bf16 v[60:63], v[196:199], v[156:159], v[60:63]\n" \
"ds_read_b128 v[184:187], v224 offset:38912\n" \
"v_mfma_f32_16x16x32_bf16 v[64:67], v[200:203], v[128:131], v[64:67]\n" \
"ds_read_b128 v[188:191], v224 offset:39168\n" \
"v_mfma_f32_16x16x32_bf16 v[68:71], v[200:203], v[132:135], v[68:71]\n" \
"v_mfma_f32_16x16x32_bf16 v[72:75], v[200:203], v[136:139], v[72:75]\n" \
"ds_read_b64_tr_b16 v[212:213], v228 offset:49152\n" \
"v_mfma_f32_16x16x32_bf16 v[76:79], v[200:203], v[140:143], v[76:79]\n" \
"ds_read_b64_tr_b16 v[214:215], v228 offset:49664\n" \
"v_mfma_f32_16x16x32_bf16 v[80:83], v[200:203], v[144:147], v[80:83]\n" \
"ds_read_b64_tr_b16 v[216:217], v226 offset:57344\n" \
"v_mfma_f32_16x16x32_bf16 v[84:87], v[200:203], v[148:151], v[84:87]\n" \
"s_add_u32 m0, %[m0w], 16384\n" \
"s_nop 0\n" \
"buffer_load_dwordx4 v231, %[rW], %[sW] offen offset:0 lds\n" \
"v_mfma_f32_16x16x32_bf16 v[88:91], v[200:203], v[152:155], v[88:91]\n" \
"ds_read_b64_tr_b16 v[218:219], v226 offset:57856\n" \
"v_mfma_f32_16x16x32_bf16 v[92:95], v[200:203], v[156:159], v[92:95]\n" \
"ds_read_b64_tr_b16 v[220:221], v228 offset:57344\n" \
"v_mfma_f32_16x16x32_bf16 v[96:99], v[204:207], v[128:131], v[96:99]\n" \
"v_mfma_f32_16x16x32_bf16 v[100:103], v[204:207], v[132:135], v[100:103]\n" \
"ds_read_b64_tr_b16 v[222:223], v228 offset:57856\n" \
"v_mfma_f32_16x16x32_bf16 v[104:107], v[204:207], v[136:139], v[104:107]\n" \
"v_mfma_f32_16x16x32_bf16 v[108:111], v[204:207], v[140:143], v[108:111]\n" \
"v_mfma_f32_16x16x32_bf16 v[112:115], v[204:207], v[144:147], v[112:115]\n" \
"buffer_load_dwordx4 v232, %[rW], %[sW] offen offset:1024 lds\n" \
"s_add_u32 %[u], %[u], 1\n" \
"s_add_u32 %[tp], %[u], %[rot]\n" \
"s_and_b32 %[tp], %[tp], 7\n" \
"s_and_b32 %[sX], %[u], 2147483640\n" \
"s_or_b32 %[tp], %[tp], %[sX]\n" \
"s_mul_i32 %[sX], %[tp], 2048\n" \
"s_mul_i32 %[sW], %[tp], 229376\n" \
"v_mfma_f32_16x16x32_bf16 v[116:119], v[204:207], v[148:151], v[116:119]\n" \
"v_mfma_f32_16x16x32_bf16 v[120:123], v[204:207], v[152:155], v[120:123]\n" \
"v_mfma_f32_16x16x32_bf16 v[124:127], v[204:207], v[156:159], v[124:127]\n" \
"s_waitcnt lgkmcnt(13)\n" \
"v_mfma_f32_16x16x32_bf16 v[0:3], v[208:211], v[160:163], v[0:3]\n" \
"s_waitcnt lgkmcnt(12)\n" \
"v_mfma_f32_16x16x32_bf16 v[4:7], v[208:211], v[164:167], v[4:7]\n" \
"s_waitcnt lgkmcnt(11)\n" \
"v_mfma_f32_16x16x32_bf16 v[8:11], v[208:211], v[168:171], v[8:11]\n" \
"s_waitcnt lgkmcnt(10)\n" \
"v_mfma_f32_16x16x32_bf16 v[12:15], v[208:211], v[172:175], v[12:15]\n" \
"s_waitcnt vmcnt(8) lgkmcnt(0)\n" \
"s_barrier\n" \
"s_sub_u32 %[cnt], %[cnt], 1\n" \
"s_cmp_lg_u32 %[cnt], 0\n" \
"s_cbranch_scc1 LOOP_%=\n" \
"v_mfma_f32_16x16x32_bf16 v[16:19], v[208:211], v[176:179], v[16:19]\n" \
"ds_read_b64_tr_b16 v[192:193], v227 offset:16384\n" \
"v_mfma_f32_16x16x32_bf16 v[20:23], v[208:211], v[180:183], v[20:23]\n" \
"ds_read_b64_tr_b16 v[194:195], v227 offset:16896\n" \
"v_mfma_f32_16x16x32_bf16 v[24:27], v[208:211], v[184:187], v[24:27]\n" \
"ds_read_b128 v[128:131], v225 offset:0\n" \
"v_mfma_f32_16x16x32_bf16 v[28:31], v[208:211], v[188:191], v[28:31]\n" \
"s_add_u32 m0, %[m0x], 32768\n" \
"s_nop 0\n" \
"buffer_load_dwordx4 v230, %[rX], %[sX] offen offset:0 sc1 lds\n" \
"v_mfma_f32_16x16x32_bf16 v[32:35], v[212:215], v[160:163], v[32:35]\n" \
"ds_read_b128 v[132:135], v225 offset:256\n" \
"v_mfma_f32_16x16x32_bf16 v[36:39], v[212:215], v[164:167], v[36:39]\n" \
"ds_read_b128 v[136:139], v225 offset:2048\n" \
"v_mfma_f32_16x16x32_bf16 v[40:43], v[212:215], v[168:171], v[40:43]\n" \
"v_mfma_f32_16x16x32_bf16 v[44:47], v[212:215], v[172:175], v[44:47]\n" \
"ds_read_b128 v[140:143], v225 offset:2304\n" \
"v_mfma_f32_16x16x32_bf16 v[48:51], v[212:215], v[176:179], v[48:51]\n" \
"ds_read_b128 v[144:147], v225 offset:4096\n" \
"v_mfma_f32_16x16x32_bf16 v[52:55], v[212:215], v[180:183], v[52:55]\n" \
"ds_read_b128 v[148:151], v225 offset:4352\n" \
"v_mfma_f32_16x16x32_bf16 v[56:59], v[212:215], v[184:187], v[56:59]\n" \
"buffer_load_dwordx4 v230, %[rX], %[sX] offen offset:1024 sc1 lds\n" \
"v_mfma_f32_16x16x32_bf16 v[60:63], v[212:215], v[188:191], v[60:63]\n" \
"ds_read_b128 v[152:155], v225 offset:6144\n" \
"v_mfma_f32_16x16x32_bf16 v[64:67], v[216:219], v[160:163], v[64:67]\n" \
"ds_read_b128 v[156:159], v225 offset:6400\n" \
"v_mfma_f32_16x16x32_bf16 v[68:71], v[216:219], v[164:167], v[68:71]\n" \
"v_mfma_f32_16x16x32_bf16 v[72:75], v[216:219], v[168:171], v[72:75]\n" \
"ds_read_b64_tr_b16 v[196:197], v229 offset:16384\n" \
"v_mfma_f32_16x16x32_bf16 v[76:79], v[216:219], v[172:175], v[76:79]\n" \
"ds_read_b64_tr_b16 v[198:199], v229 offset:16896\n" \
"v_mfma_f32_16x16x32_bf16 v[80:83], v[216:219], v[176:179], v[80:83]\n" \
"ds_read_b64_tr_b16 v[200:201], v227 offset:24576\n" \
"v_mfma_f32_16x16x32_bf16 v[84:87], v[216:219], v[180:183], v[84:87]\n" \
"s_add_u32 m0, %[m0w], 49152\n" \
"s_nop 0\n" \
"buffer_load_dwordx4 v231, %[rW], %[sW] offen offset:0 lds\n" \
"v_mfma_f32_16x16x32_bf16 v[88:91], v[216:219], v[184:187], v[88:91]\n" \
"ds_read_b64_tr_b16 v[202:203], v227 offset:25088\n" \
"v_mfma_f32_16x16x32_bf16 v[92:95], v[216:219], v[188:191], v[92:95]\n" \
"ds_read_b64_tr_b16 v[204:205], v229 offset:24576\n" \
"v_mfma_f32_16x16x32_bf16 v[96:99], v[220:223], v[160:163], v[96:99]\n" \
"v_mfma_f32_16x16x32_bf16 v[100:103], v[220:223], v[164:167], v[100:103]\n" \
"ds_read_b64_tr_b16 v[206:207], v229 offset:25088\n" \
"v_mfma_f32_16x16x32_bf16 v[104:107], v[220:223], v[168:171], v[104:107]\n" \
"v_mfma_f32_16x16x32_bf16 v[108:111], v[220:223], v[172:175], v[108:111]\n" \
"v_mfma_f32_16x16x32_bf16 v[112:115], v[220:223], v[176:179], v[112:115]\n" \
"buffer_load_dwordx4 v232, %[rW], %[sW] offen offset:1024 lds\n" \
"s_add_u32 %[u], %[u], 1\n" \
"s_add_u32 %[tp], %[u], %[rot]\n" \
"s_and_b32 %[tp], %[tp], 7\n" \
"s_and_b32 %[sX], %[u], 2147483640\n" \
"s_or_b32 %[tp], %[tp], %[sX]\n" \
"s_mul_i32 %[sX], %[tp], 2048\n" \
"s_mul_i32 %[sW], %[tp], 229376\n" \
"v_mfma_f32_16x16x32_bf16 v[116:119], v[220:223], v[180:183], v[116:119]\n" \
"v_mfma_f32_16x16x32_bf16 v[120:123], v[220:223], v[184:187], v[120:123]\n" \
"v_mfma_f32_16x16x32_bf16 v[124:127], v[220:223], v[188:191], v[124:127]\n" \
"s_waitcnt lgkmcnt(13)\n" \
"v_mfma_f32_16x16x32_bf16 v[0:3], v[192:195], v[128:131], v[0:3]\n" \
"s_waitcnt lgkmcnt(12)\n" \
"v_mfma_f32_16x16x32_bf16 v[4:7], v[192:195], v[132:135], v[4:7]\n" \
"s_waitcnt lgkmcnt(11)\n" \
"v_mfma_f32_16x16x32_bf16 v[8:11], v[192:195], v[136:139], v[8:11]\n" \
"s_waitcnt lgkmcnt(10)\n" \
"v_mfma_f32_16x16x32_bf16 v[12:15], v[192:195], v[140:143], v[12:15]\n" \
"s_waitcnt vmcnt(8) lgkmcnt(0)\n" \
"s_barrier\n" \
"v_mfma_f32_16x16x32_bf16 v[16:19], v[192:195], v[144:147], v[16:19]\n" \
"ds_read_b64_tr_b16 v[208:209], v227 offset:49152\n" \
"v_mfma_f32_16x16x32_bf16 v[20:23], v[192:195], v[148:151], v[20:23]\n" \
"ds_read_b64_tr_b16 v[210:211], v227 offset:49664\n" \
"v_mfma_f32_16x16x32_bf16 v[24:27], v[192:195], v[152:155], v[24:27]\n" \
"ds_read_b128 v[160:163], v225 offset:32768\n" \
"v_mfma_f32_16x16x32_bf16 v[28:31], v[192:195], v[156:159], v[28:31]\n" \
"s_add_u32 m0, %[m0x], 65536\n" \
"s_nop 0\n" \
"buffer_load_dwordx4 v230, %[rX], %[sX] offen offset:0 sc1 lds\n" \
"v_mfma_f32_16x16x32_bf16 v[32:35], v[196:199], v[128:131], v[32:35]\n" \
"ds_read_b128 v[164:167], v225 offset:33024\n" \
"v_mfma_f32_16x16x32_bf16 v[36:39], v[196:199], v[132:135], v[36:39]\n" \
"ds_read_b128 v[168:171], v225 offset:34816\n" \
"v_mfma_f32_16x16x32_bf16 v[40:43], v[196:199], v[136:139], v[40:43]\n" \
"v_mfma_f32_16x16x32_bf16 v[44:47], v[196:199], v[140:143], v[44:47]\n" \
"ds_read_b128 v[172:175], v225 offset:35072\n" \
"v_mfma_f32_16x16x32_bf16 v[48:51], v[196:199], v[144:147], v[48:51]\n" \
"ds_read_b128 v[176:179], v225 offset:36864\n" \
"v_mfma_f32_16x16x32_bf16 v[52:55], v[196:199], v[148:151], v[52:55]\n" \
"ds_read_b128 v[180:183], v225 offset:37120\n" \
"v_mfma_f32_16x16x32_bf16 v[56:59], v[196:199], v[152:155], v[56:59]\n" \
"buffer_load_dwordx4 v230, %[rX], %[sX] offen offset:1024 sc1 lds\n" \
"v_mfma_f32_16x16x32_bf16 v[60:63], v[196:199], v[156:159], v[60:63]\n" \
"ds_read_b128 v[184:187], v225 offset:38912\n" \
"v_mfma_f32_16x16x32_bf16 v[64:67], v[200:203], v[128:131], v[64:67]\n" \
"ds_read_b128 v[188:191], v225 offset:39168\n" \
"v_mfma_f32_16x16x32_bf16 v[68:71], v[200:203], v[132:135], v[68:71]\n" \
"v_mfma_f32_16x16x32_bf16 v[72:75], v[200:203], v[136:139], v[72:75]\n" \
"ds_read_b64_tr_b16 v[212:213], v229 offset:49152\n" \
"v_mfma_f32_16x16x32_bf16 v[76:79], v[200:203], v[140:143], v[76:79]\n" \
"ds_read_b64_tr_b16 v[214:215], v229 offset:49664\n" \
"v_mfma_f32_16x16x32_bf16 v[80:83], v[200:203], v[144:147], v[80:83]\n" \
"ds_read_b64_tr_b16 v[216:217], v227 offset:57344\n" \
"v_mfma_f32_16x16x32_bf16 v[84:87], v[200:203], v[148:151], v[84:87]\n" \
"s_add_u32 m0, %[m0w], 81920\n" \
"s_nop 0\n" \
"buffer_load_dwordx4 v231, %[rW], %[sW] offen offset:0 lds\n" \
"v_mfma_f32_16x16x32_bf16 v[88:91], v[200:203], v[152:155], v[88:91]\n" \
"ds_read_b64_tr_b16 v[218:219], v227 offset:57856\n" \
"v_mfma_f32_16x16x32_bf16 v[92:95], v[200:203], v[156:159], v[92:95]\n" \
"ds_read_b64_tr_b16 v[220:221], v229 offset:57344\n" \
"v_mfma_f32_16x16x32_bf16 v[96:99], v[204:207], v[128:131], v[96:99]\n" \
"v_mfma_f32_16x16x32_bf16 v[100:103], v[204:207], v[132:135], v[100:103]\n" \
"ds_read_b64_tr_b16 v[222:223], v229 offset:57856\n" \
"v_mfma_f32_16x16x32_bf16 v[104:107], v[204:207], v[136:139], v[104:107]\n" \
"v_mfma_f32_16x16x32_bf16 v[108:111], v[204:207], v[140:143], v[108:111]\n" \
"v_mfma_f32_16x16x32_bf16 v[112:115], v[204:207], v[144:147], v[112:115]\n" \
"buffer_load_dwordx4 v232, %[rW], %[sW] offen offset:1024 lds\n" \
"s_add_u32 %[u], %[u], 1\n" \
"s_add_u32 %[tp], %[u], %[rot]\n" \
"s_and_b32 %[tp], %[tp], 7\n" \
"s_and_b32 %[sX], %[u], 2147483640\n" \
"s_or_b32 %[tp], %[tp], %[sX]\n" \
"s_mul_i32 %[sX], %[tp], 2048\n" \
"s_mul_i32 %[sW], %[tp], 229376\n" \
"v_mfma_f32_16x16x32_bf16 v[116:119], v[204:207], v[148:151], v[116:119]\n" \
"v_mfma_f32_16x16x32_bf16 v[120:123], v[204:207], v[152:155], v[120:123]\n" \
"v_mfma_f32_16x16x32_bf16 v[124:127], v[204:207], v[156:159], v[124:127]\n" \
"s_waitcnt lgkmcnt(13)\n" \
"v_mfma_f32_16x16x32_bf16 v[0:3], v[208:211], v[160:163], v[0:3]\n" \
"s_waitcnt lgkmcnt(12)\n" \
"v_mfma_f32_16x16x32_bf16 v[4:7], v[208:211], v[164:167], v[4:7]\n" \
"s_waitcnt lgkmcnt(11)\n" \
"v_mfma_f32_16x16x32_bf16 v[8:11], v[208:211], v[168:171], v[8:11]\n" \
"s_waitcnt lgkmcnt(10)\n" \
"v_mfma_f32_16x16x32_bf16 v[12:15], v[208:211], v[172:175], v[12:15]\n" \
"s_waitcnt vmcnt(8) lgkmcnt(0)\n" \
"s_barrier\n" \
"v_mfma_f32_16x16x32_bf16 v[16:19], v[208:211], v[176:179], v[16:19]\n" \
"ds_read_b64_tr_b16 v[192:193], v226 offset:16384\n" \
"v_mfma_f32_16x16x32_bf16 v[20:23], v[208:211], v[180:183], v[20:23]\n" \
"ds_read_b64_tr_b16 v[194:195], v226 offset:16896\n" \
"v_mfma_f32_16x16x32_bf16 v[24:27], v[208:211], v[184:187], v[24:27]\n" \
"ds_read_b128 v[128:131], v224 offset:0\n" \
"v_mfma_f32_16x16x32_bf16 v[28:31], v[208:211], v[188:191], v[28:31]\n" \
"s_add_u32 m0, %[m0x], 98304\n" \
"s_nop 0\n" \
"buffer_load_dwordx4 v230, %[rX], %[sX] offen offset:0 sc1 lds\n" \
"v_mfma_f32_16x16x32_bf16 v[32:35], v[212:215], v[160:163], v[32:35]\n" \
"ds_read_b128 v[132:135], v224 offset:256\n" \
"v_mfma_f32_16x16x32_bf16 v[36:39], v[212:215], v[164:167], v[36:39]\n" \
"ds_read_b128 v[136:139], v224 offset:2048\n" \
"v_mfma_f32_16x16x32_bf16 v[40:43], v[212:215], v[168:171], v[40:43]\n" \
"v_mfma_f32_16x16x32_bf16 v[44:47], v[212:215], v[172:175], v[44:47]\n" \
"ds_read_b128 v[140:143], v224 offset:2304\n" \
"v_mfma_f32_16x16x32_bf16 v[48:51], v[212:215], v[176:179], v[48:51]\n" \
"ds_read_b128 v[144:147], v224 offset:4096\n" \
"v_mfma_f32_16x16x32_bf16 v[52:55], v[212:215], v[180:183], v[52:55]\n" \
"ds_read_b128 v[148:151], v224 offset:4352\n" \
"v_mfma_f32_16x16x32_bf16 v[56:59], v[212:215], v[184:187], v[56:59]\n" \
"buffer_load_dwordx4 v230, %[rX], %[sX] offen offset:1024 sc1 lds\n" \
"v_mfma_f32_16x16x32_bf16 v[60:63], v[212:215], v[188:191], v[60:63]\n" \
"ds_read_b128 v[152:155], v224 offset:6144\n" \
"v_mfma_f32_16x16x32_bf16 v[64:67], v[216:219], v[160:163], v[64:67]\n" \
"ds_read_b128 v[156:159], v224 offset:6400\n" \
"v_mfma_f32_16x16x32_bf16 v[68:71], v[216:219], v[164:167], v[68:71]\n" \
"v_mfma_f32_16x16x32_bf16 v[72:75], v[216:219], v[168:171], v[72:75]\n" \
"ds_read_b64_tr_b16 v[196:197], v228 offset:16384\n" \
"v_mfma_f32_16x16x32_bf16 v[76:79], v[216:219], v[172:175], v[76:79]\n" \
"ds_read_b64_tr_b16 v[198:199], v228 offset:16896\n" \
"v_mfma_f32_16x16x32_bf16 v[80:83], v[216:219], v[176:179], v[80:83]\n" \
"ds_read_b64_tr_b16 v[200:201], v226 offset:24576\n" \
"v_mfma_f32_16x16x32_bf16 v[84:87], v[216:219], v[180:183], v[84:87]\n" \
"s_add_u32 m0, %[m0w], 114688\n" \
"s_nop 0\n" \
"buffer_load_dwordx4 v231, %[rW], %[sW] offen offset:0 lds\n" \
"v_mfma_f32_16x16x32_bf16 v[88:91], v[216:219], v[184:187], v[88:91]\n" \
"ds_read_b64_tr_b16 v[202:203], v226 offset:25088\n" \
"v_mfma_f32_16x16x32_bf16 v[92:95], v[216:219], v[188:191], v[92:95]\n" \
"ds_read_b64_tr_b16 v[204:205], v228 offset:24576\n" \
"v_mfma_f32_16x16x32_bf16 v[96:99], v[220:223], v[160:163], v[96:99]\n" \
"v_mfma_f32_16x16x32_bf16 v[100:103], v[220:223], v[164:167], v[100:103]\n" \
"ds_read_b64_tr_b16 v[206:207], v228 offset:25088\n" \
"v_mfma_f32_16x16x32_bf16 v[104:107], v[220:223], v[168:171], v[104:107]\n" \
"v_mfma_f32_16x16x32_bf16 v[108:111], v[220:223], v[172:175], v[108:111]\n" \
"v_mfma_f32_16x16x32_bf16 v[112:115], v[220:223], v[176:179], v[112:115]\n" \
"buffer_load_dwordx4 v232, %[rW], %[sW] offen offset:1024 lds\n" \
"s_add_u32 %[u], %[u], 1\n" \
"s_add_u32 %[tp], %[u], %[rot]\n" \
"s_and_b32 %[tp], %[tp], 7\n" \
"s_and_b32 %[sX], %[u], 2147483640\n" \
"s_or_b32 %[tp], %[tp], %[sX]\n" \
"s_mul_i32 %[sX], %[tp], 2048\n" \
"s_mul_i32 %[sW], %[tp], 229376\n" \
"v_mfma_f32_16x16x32_bf16 v[116:119], v[220:223], v[180:183], v[116:119]\n" \
"v_mfma_f32_16x16x32_bf16 v[120:123], v[220:223], v[184:187], v[120:123]\n" \
"v_mfma_f32_16x16x32_bf16 v[124:127], v[220:223], v[188:191], v[124:127]\n" \
"s_waitcnt lgkmcnt(13)\n" \
"v_mfma_f32_16x16x32_bf16 v[0:3], v[192:195], v[128:131], v[0:3]\n" \
"s_waitcnt lgkmcnt(12)\n" \
"v_mfma_f32_16x16x32_bf16 v[4:7], v[192:195], v[132:135], v[4:7]\n" \
"s_waitcnt lgkmcnt(11)\n" \
"v_mfma_f32_16x16x32_bf16 v[8:11], v[192:195], v[136:139], v[8:11]\n" \
"s_waitcnt lgkmcnt(10)\n" \
"v_mfma_f32_16x16x32_bf16 v[12:15], v[192:195], v[140:143], v[12:15]\n" \
"s_waitcnt vmcnt(8) lgkmcnt(0)\n" \
"s_barrier\n" \
"v_mfma_f32_16x16x32_bf16 v[16:19], v[192:195], v[144:147], v[16:19]\n" \
"ds_read_b64_tr_b16 v[208:209], v226 offset:49152\n" \
"v_mfma_f32_16x16x32_bf16 v[20:23], v[192:195], v[148:151], v[20:23]\n" \
"ds_read_b64_tr_b16 v[210:211], v226 offset:49664\n" \
"v_mfma_f32_16x16x32_bf16 v[24:27], v[192:195], v[152:155], v[24:27]\n" \
"ds_read_b128 v[160:163], v224 offset:32768\n" \
"v_mfma_f32_16x16x32_bf16 v[28:31], v[192:195], v[156:159], v[28:31]\n" \
"v_mfma_f32_16x16x32_bf16 v[32:35], v[196:199], v[128:131], v[32:35]\n" \
"ds_read_b128 v[164:167], v224 offset:33024\n" \
"v_mfma_f32_16x16x32_bf16 v[36:39], v[196:199], v[132:135], v[36:39]\n" \
"ds_read_b128 v[168:171], v224 offset:34816\n" \
"v_mfma_f32_16x16x32_bf16 v[40:43], v[196:199], v[136:139], v[40:43]\n" \
"v_mfma_f32_16x16x32_bf16 v[44:47], v[196:199], v[140:143], v[44:47]\n" \
"ds_read_b128 v[172:175], v224 offset:35072\n" \
"v_mfma_f32_16x16x32_bf16 v[48:51], v[196:199], v[144:147], v[48:51]\n" \
"ds_read_b128 v[176:179], v224 offset:36864\n" \
"v_mfma_f32_16x16x32_bf16 v[52:55], v[196:199], v[148:151], v[52:55]\n" \
"ds_read_b128 v[180:183], v224 offset:37120\n" \
"v_mfma_f32_16x16x32_bf16 v[56:59], v[196:199], v[152:155], v[56:59]\n" \
"v_mfma_f32_16x16x32_bf16 v[60:63], v[196:199], v[156:159], v[60:63]\n" \
"ds_read_b128 v[184:187], v224 offset:38912\n" \
"v_mfma_f32_16x16x32_bf16 v[64:67], v[200:203], v[128:131], v[64:67]\n" \
"ds_read_b128 v[188:191], v224 offset:39168\n" \
"v_mfma_f32_16x16x32_bf16 v[68:71], v[200:203], v[132:135], v[68:71]\n" \
"v_mfma_f32_16x16x32_bf16 v[72:75], v[200:203], v[136:139], v[72:75]\n" \
"ds_read_b64_tr_b16 v[212:213], v228 offset:49152\n" \
"v_mfma_f32_16x16x32_bf16 v[76:79], v[200:203], v[140:143], v[76:79]\n" \
"ds_read_b64_tr_b16 v[214:215], v228 offset:49664\n" \
"v_mfma_f32_16x16x32_bf16 v[80:83], v[200:203], v[144:147], v[80:83]\n" \
"ds_read_b64_tr_b16 v[216:217], v226 offset:57344\n" \
"v_mfma_f32_16x16x32_bf16 v[84:87], v[200:203], v[148:151], v[84:87]\n" \
"v_mfma_f32_16x16x32_bf16 v[88:91], v[200:203], v[152:155], v[88:91]\n" \
"ds_read_b64_tr_b16 v[218:219], v226 offset:57856\n" \
"v_mfma_f32_16x16x32_bf16 v[92:95], v[200:203], v[156:159], v[92:95]\n" \
"ds_read_b64_tr_b16 v[220:221], v228 offset:57344\n" \
"v_mfma_f32_16x16x32_bf16 v[96:99], v[204:207], v[128:131], v[96:99]\n" \
"v_mfma_f32_16x16x32_bf16 v[100:103], v[204:207], v[132:135], v[100:103]\n" \
"ds_read_b64_tr_b16 v[222:223], v228 offset:57856\n" \
"v_mfma_f32_16x16x32_bf16 v[104:107], v[204:207], v[136:139], v[104:107]\n" \
"v_mfma_f32_16x16x32_bf16 v[108:111], v[204:207], v[140:143], v[108:111]\n" \
"v_mfma_f32_16x16x32_bf16 v[112:115], v[204:207], v[144:147], v[112:115]\n" \
"v_mfma_f32_16x16x32_bf16 v[116:119], v[204:207], v[148:151], v[116:119]\n" \
"v_mfma_f32_16x16x32_bf16 v[120:123], v[204:207], v[152:155], v[120:123]\n" \
"v_mfma_f32_16x16x32_bf16 v[124:127], v[204:207], v[156:159], v[124:127]\n" \
"s_waitcnt lgkmcnt(13)\n" \
"v_mfma_f32_16x16x32_bf16 v[0:3], v[208:211], v[160:163], v[0:3]\n" \
"s_waitcnt lgkmcnt(12)\n" \
"v_mfma_f32_16x16x32_bf16 v[4:7], v[208:211], v[164:167], v[4:7]\n" \
"s_waitcnt lgkmcnt(11)\n" \
"v_mfma_f32_16x16x32_bf16 v[8:11], v[208:211], v[168:171], v[8:11]\n" \
"s_waitcnt lgkmcnt(10)\n" \
"v_mfma_f32_16x16x32_bf16 v[12:15], v[208:211], v[172:175], v[12:15]\n" \
"s_waitcnt vmcnt(4) lgkmcnt(0)\n" \
"s_barrier\n" \
"v_mfma_f32_16x16x32_bf16 v[16:19], v[208:211], v[176:179], v[16:19]\n" \
"ds_read_b64_tr_b16 v[192:193], v227 offset:16384\n" \
"v_mfma_f32_16x16x32_bf16 v[20:23], v[208:211], v[180:183], v[20:23]\n" \
"ds_read_b64_tr_b16 v[194:195], v227 offset:16896\n" \
"v_mfma_f32_16x16x32_bf16 v[24:27], v[208:211], v[184:187], v[24:27]\n" \
"ds_read_b128 v[128:131], v225 offset:0\n" \
"v_mfma_f32_16x16x32_bf16 v[28:31], v[208:211], v[188:191], v[28:31]\n" \
"v_mfma_f32_16x16x32_bf16 v[32:35], v[212:215], v[160:163], v[32:35]\n" \
"ds_read_b128 v[132:135], v225 offset:256\n" \
"v_mfma_f32_16x16x32_bf16 v[36:39], v[212:215], v[164:167], v[36:39]\n" \
"ds_read_b128 v[136:139], v225 offset:2048\n" \
"v_mfma_f32_16x16x32_bf16 v[40:43], v[212:215], v[168:171], v[40:43]\n" \
"v_mfma_f32_16x16x32_bf16 v[44:47], v[212:215], v[172:175], v[44:47]\n" \
"ds_read_b128 v[140:143], v225 offset:2304\n" \
"v_mfma_f32_16x16x32_bf16 v[48:51], v[212:215], v[176:179], v[48:51]\n" \
"ds_read_b128 v[144:147], v225 offset:4096\n" \
"v_mfma_f32_16x16x32_bf16 v[52:55], v[212:215], v[180:183], v[52:55]\n" \
"ds_read_b128 v[148:151], v225 offset:4352\n" \
"v_mfma_f32_16x16x32_bf16 v[56:59], v[212:215], v[184:187], v[56:59]\n" \
"v_mfma_f32_16x16x32_bf16 v[60:63], v[212:215], v[188:191], v[60:63]\n" \
"ds_read_b128 v[152:155], v225 offset:6144\n" \
"v_mfma_f32_16x16x32_bf16 v[64:67], v[216:219], v[160:163], v[64:67]\n" \
"ds_read_b128 v[156:159], v225 offset:6400\n" \
"v_mfma_f32_16x16x32_bf16 v[68:71], v[216:219], v[164:167], v[68:71]\n" \
"v_mfma_f32_16x16x32_bf16 v[72:75], v[216:219], v[168:171], v[72:75]\n" \
"ds_read_b64_tr_b16 v[196:197], v229 offset:16384\n" \
"v_mfma_f32_16x16x32_bf16 v[76:79], v[216:219], v[172:175], v[76:79]\n" \
"ds_read_b64_tr_b16 v[198:199], v229 offset:16896\n" \
"v_mfma_f32_16x16x32_bf16 v[80:83], v[216:219], v[176:179], v[80:83]\n" \
"ds_read_b64_tr_b16 v[200:201], v227 offset:24576\n" \
"v_mfma_f32_16x16x32_bf16 v[84:87], v[216:219], v[180:183], v[84:87]\n" \
"v_mfma_f32_16x16x32_bf16 v[88:91], v[216:219], v[184:187], v[88:91]\n" \
"ds_read_b64_tr_b16 v[202:203], v227 offset:25088\n" \
"v_mfma_f32_16x16x32_bf16 v[92:95], v[216:219], v[188:191], v[92:95]\n" \
"ds_read_b64_tr_b16 v[204:205], v229 offset:24576\n" \
"v_mfma_f32_16x16x32_bf16 v[96:99], v[220:223], v[160:163], v[96:99]\n" \
"v_mfma_f32_16x16x32_bf16 v[100:103], v[220:223], v[164:167], v[100:103]\n" \
"ds_read_b64_tr_b16 v[206:207], v229 offset:25088\n" \
"v_mfma_f32_16x16x32_bf16 v[104:107], v[220:223], v[168:171], v[104:107]\n" \
"v_mfma_f32_16x16x32_bf16 v[108:111], v[220:223], v[172:175], v[108:111]\n" \
"v_mfma_f32_16x16x32_bf16 v[112:115], v[220:223], v[176:179], v[112:115]\n" \
"v_mfma_f32_16x16x32_bf16 v[116:119], v[220:223], v[180:183], v[116:119]\n" \
"v_mfma_f32_16x16x32_bf16 v[120:123], v[220:223], v[184:187], v[120:123]\n" \
"v_mfma_f32_16x16x32_bf16 v[124:127], v[220:223], v[188:191], v[124:127]\n" \
"s_waitcnt lgkmcnt(13)\n" \
"v_mfma_f32_16x16x32_bf16 v[0:3], v[192:195], v[128:131], v[0:3]\n" \
"s_waitcnt lgkmcnt(12)\n" \
"v_mfma_f32_16x16x32_bf16 v[4:7], v[192:195], v[132:135], v[4:7]\n" \
"s_waitcnt lgkmcnt(11)\n" \
"v_mfma_f32_16x16x32_bf16 v[8:11], v[192:195], v[136:139], v[8:11]\n" \
"s_waitcnt lgkmcnt(10)\n" \
"v_mfma_f32_16x16x32_bf16 v[12:15], v[192:195], v[140:143], v[12:15]\n" \
"s_waitcnt vmcnt(0) lgkmcnt(0)\n" \
"s_barrier\n" \
"v_mfma_f32_16x16x32_bf16 v[16:19], v[192:195], v[144:147], v[16:19]\n" \
"ds_read_b64_tr_b16 v[208:209], v227 offset:49152\n" \
"v_mfma_f32_16x16x32_bf16 v[20:23], v[192:195], v[148:151], v[20:23]\n" \
"ds_read_b64_tr_b16 v[210:211], v227 offset:49664\n" \
"v_mfma_f32_16x16x32_bf16 v[24:27], v[192:195], v[152:155], v[24:27]\n" \
"ds_read_b128 v[160:163], v225 offset:32768\n" \
"v_mfma_f32_16x16x32_bf16 v[28:31], v[192:195], v[156:159], v[28:31]\n" \
"v_mfma_f32_16x16x32_bf16 v[32:35], v[196:199], v[128:131], v[32:35]\n" \
"ds_read_b128 v[164:167], v225 offset:33024\n" \
"v_mfma_f32_16x16x32_bf16 v[36:39], v[196:199], v[132:135], v[36:39]\n" \
"ds_read_b128 v[168:171], v225 offset:34816\n" \
"v_mfma_f32_16x16x32_bf16 v[40:43], v[196:199], v[136:139], v[40:43]\n" \
"v_mfma_f32_16x16x32_bf16 v[44:47], v[196:199], v[140:143], v[44:47]\n" \
"ds_read_b128 v[172:175], v225 offset:35072\n" \
"v_mfma_f32_16x16x32_bf16 v[48:51], v[196:199], v[144:147], v[48:51]\n" \
"ds_read_b128 v[176:179], v225 offset:36864\n" \
"v_mfma_f32_16x16x32_bf16 v[52:55], v[196:199], v[148:151], v[52:55]\n" \
"ds_read_b128 v[180:183], v225 offset:37120\n" \
"v_mfma_f32_16x16x32_bf16 v[56:59], v[196:199], v[152:155], v[56:59]\n" \
"v_mfma_f32_16x16x32_bf16 v[60:63], v[196:199], v[156:159], v[60:63]\n" \
"ds_read_b128 v[184:187], v225 offset:38912\n" \
"v_mfma_f32_16x16x32_bf16 v[64:67], v[200:203], v[128:131], v[64:67]\n" \
"ds_read_b128 v[188:191], v225 offset:39168\n" \
"v_mfma_f32_16x16x32_bf16 v[68:71], v[200:203], v[132:135], v[68:71]\n" \
"v_mfma_f32_16x16x32_bf16 v[72:75], v[200:203], v[136:139], v[72:75]\n" \
"ds_read_b64_tr_b16 v[212:213], v229 offset:49152\n" \
"v_mfma_f32_16x16x32_bf16 v[76:79], v[200:203], v[140:143], v[76:79]\n" \
"ds_read_b64_tr_b16 v[214:215], v229 offset:49664\n" \
"v_mfma_f32_16x16x32_bf16 v[80:83], v[200:203], v[144:147], v[80:83]\n" \
"ds_read_b64_tr_b16 v[216:217], v227 offset:57344\n" \
"v_mfma_f32_16x16x32_bf16 v[84:87], v[200:203], v[148:151], v[84:87]\n" \
"v_mfma_f32_16x16x32_bf16 v[88:91], v[200:203], v[152:155], v[88:91]\n" \
"ds_read_b64_tr_b16 v[218:219], v227 offset:57856\n" \
"v_mfma_f32_16x16x32_bf16 v[92:95], v[200:203], v[156:159], v[92:95]\n" \
"ds_read_b64_tr_b16 v[220:221], v229 offset:57344\n" \
"v_mfma_f32_16x16x32_bf16 v[96:99], v[204:207], v[128:131], v[96:99]\n" \
"v_mfma_f32_16x16x32_bf16 v[100:103], v[204:207], v[132:135], v[100:103]\n" \
"ds_read_b64_tr_b16 v[222:223], v229 offset:57856\n" \
"v_mfma_f32_16x16x32_bf16 v[104:107], v[204:207], v[136:139], v[104:107]\n" \
"v_mfma_f32_16x16x32_bf16 v[108:111], v[204:207], v[140:143], v[108:111]\n" \
"v_mfma_f32_16x16x32_bf16 v[112:115], v[204:207], v[144:147], v[112:115]\n" \
"v_mfma_f32_16x16x32_bf16 v[116:119], v[204:207], v[148:151], v[116:119]\n" \
"v_mfma_f32_16x16x32_bf16 v[120:123], v[204:207], v[152:155], v[120:123]\n" \
"v_mfma_f32_16x16x32_bf16 v[124:127], v[204:207], v[156:159], v[124:127]\n" \
"s_waitcnt lgkmcnt(13)\n" \
"v_mfma_f32_16x16x32_bf16 v[0:3], v[208:211], v[160:163], v[0:3]\n" \
"s_waitcnt lgkmcnt(12)\n" \
"v_mfma_f32_16x16x32_bf16 v[4:7], v[208:211], v[164:167], v[4:7]\n" \
"s_waitcnt lgkmcnt(11)\n" \
"v_mfma_f32_16x16x32_bf16 v[8:11], v[208:211], v[168:171], v[8:11]\n" \
"s_waitcnt lgkmcnt(10)\n" \
"v_mfma_f32_16x16x32_bf16 v[12:15], v[208:211], v[172:175], v[12:15]\n" \
"s_waitcnt lgkmcnt(9)\n" \
"v_mfma_f32_16x16x32_bf16 v[16:19], v[208:211], v[176:179], v[16:19]\n" \
"s_waitcnt lgkmcnt(8)\n" \
"v_mfma_f32_16x16x32_bf16 v[20:23], v[208:211], v[180:183], v[20:23]\n" \
"s_waitcnt lgkmcnt(7)\n" \
"v_mfma_f32_16x16x32_bf16 v[24:27], v[208:211], v[184:187], v[24:27]\n" \
"s_waitcnt lgkmcnt(6)\n" \
"v_mfma_f32_16x16x32_bf16 v[28:31], v[208:211], v[188:191], v[28:31]\n" \
"s_waitcnt lgkmcnt(4)\n" \
"v_mfma_f32_16x16x32_bf16 v[32:35], v[212:215], v[160:163], v[32:35]\n" \
"v_mfma_f32_16x16x32_bf16 v[36:39], v[212:215], v[164:167], v[36:39]\n" \
"v_mfma_f32_16x16x32_bf16 v[40:43], v[212:215], v[168:171], v[40:43]\n" \
"v_mfma_f32_16x16x32_bf16 v[44:47], v[212:215], v[172:175], v[44:47]\n" \
"v_mfma_f32_16x16x32_bf16 v[48:51], v[212:215], v[176:179], v[48:51]\n" \
"v_mfma_f32_16x16x32_bf16 v[52:55], v[212:215], v[180:183], v[52:55]\n" \
"v_mfma_f32_16x16x32_bf16 v[56:59], v[212:215], v[184:187], v[56:59]\n" \
"v_mfma_f32_16x16x32_bf16 v[60:63], v[212:215], v[188:191], v[60:63]\n" \
"s_waitcnt lgkmcnt(2)\n" \
"v_mfma_f32_16x16x32_bf16 v[64:67], v[216:219], v[160:163], v[64:67]\n" \
"v_mfma_f32_16x16x32_bf16 v[68:71], v[216:219], v[164:167], v[68:71]\n" \
"v_mfma_f32_16x16x32_bf16 v[72:75], v[216:219], v[168:171], v[72:75]\n" \
"v_mfma_f32_16x16x32_bf16 v[76:79], v[216:219], v[172:175], v[76:79]\n" \
"v_mfma_f32_16x16x32_bf16 v[80:83], v[216:219], v[176:179], v[80:83]\n" \
"v_mfma_f32_16x16x32_bf16 v[84:87], v[216:219], v[180:183], v[84:87]\n" \
"v_mfma_f32_16x16x32_bf16 v[88:91], v[216:219], v[184:187], v[88:91]\n" \
"v_mfma_f32_16x16x32_bf16 v[92:95], v[216:219], v[188:191], v[92:95]\n" \
"s_waitcnt lgkmcnt(0)\n" \
"v_mfma_f32_16x16x32_bf16 v[96:99], v[220:223], v[160:163], v[96:99]\n" \
"v_mfma_f32_16x16x32_bf16 v[100:103], v[220:223], v[164:167], v[100:103]\n" \
"v_mfma_f32_16x16x32_bf16 v[104:107], v[220:223], v[168:171], v[104:107]\n" \
"v_mfma_f32_16x16x32_bf16 v[108:111], v[220:223], v[172:175], v[108:111]\n" \
"v_mfma_f32_16x16x32_bf16 v[112:115], v[220:223], v[176:179], v[112:115]\n" \
"v_mfma_f32_16x16x32_bf16 v[116:119], v[220:223], v[180:183], v[116:119]\n" \
"v_mfma_f32_16x16x32_bf16 v[120:123], v[220:223], v[184:187], v[120:123]\n" \
"v_mfma_f32_16x16x32_bf16 v[124:127], v[220:223], v[188:191], v[124:127]\n" \
"s_nop 15\n" \
"s_nop 7\n" \
    : "={v[0:15]}"(acc[0]), "={v[16:31]}"(acc[1]), "={v[32:47]}"(acc[2]), "={v[48:63]}"(acc[3]), "={v[64:79]}"(acc[4]), "={v[80:95]}"(acc[5]), "={v[96:111]}"(acc[6]), "={v[112:127]}"(acc[7]), [sX] "+&s"(sX_), [sW] "+&s"(sW_), [cnt] "=&s"(cnt_), [u] "+&s"(u_), [tp] "=&s"(tp_) : "{v230}"(vxd[0]), "{v231}"(vwd[0]), "{v232}"(vwd[1]), "{v224}"(vxr[0] + 0u), "{v225}"(vxr[0] + 65536u), "{v226}"(vwr[0] + 0u), "{v227}"(vwr[0] + 65536u), "{v228}"(vwr[1] + 0u), "{v229}"(vwr[1] + 65536u), [rX] "s"(rX_), [rW] "s"(rW_), [m0x] "s"(m0x_), [m0w] "s"(m0w_), [rot] "s"(rot_) : "memory", "scc", "v128", "v129", "v130", "v131", "v132", "v133", "v134", "v135", "v136", "v137", "v138", "v139", "v140", "v141", "v142", "v143", "v144", "v145", "v146", "v147", "v148", "v149", "v150", "v151", "v152", "v153", "v154", "v155", "v156", "v157", "v158", "v159", "v192", "v193", "v194", "v195", "v196", "v197", "v198", "v199", "v200", "v201", "v202", "v203", "v204", "v205", "v206", "v207", "v160", "v161", "v162", "v163", "v164", "v165", "v166", "v167", "v168", "v169", "v170", "v171", "v172", "v173", "v174", "v175", "v176", "v177", "v178", "v179", "v180", "v181", "v182", "v183", "v184", "v185", "v186", "v187", "v188", "v189", "v190", "v191", "v208", "v209", "v210", "v211", "v212", "v213", "v214", "v215", "v216", "v217", "v218", "v219", "v220", "v221", "v222", "v223")

#define QKV_STAGE 40960
#define QKV_LDS 122880
#define QKV_PROW(vwd, rW_, m0w_, s0_, s1_) asm volatile( \
"s_add_u32 m0, %[m0w], 16384\n" \
"s_nop 0\n" \
"buffer_load_dwordx4 v183, %[rW], %[s0] offen offset:0 lds\n" \
"buffer_load_dwordx4 v184, %[rW], %[s0] offen offset:1024 lds\n" \
"buffer_load_dwordx4 v185, %[rW], %[s0] offen offset:2048 lds\n" \
"s_add_u32 m0, %[m0w], 57344\n" \
"s_nop 0\n" \
"buffer_load_dwordx4 v183, %[rW], %[s1] offen offset:0 lds\n" \
"buffer_load_dwordx4 v184, %[rW], %[s1] offen offset:1024 lds\n" \
"buffer_load_dwordx4 v185, %[rW], %[s1] offen offset:2048 lds\n" \
    :: "{v183}"(vwd[0]), "{v184}"(vwd[1]), "{v185}"(vwd[2]), [rW] "s"(rW_), [m0w] "s"(m0w_), [s0] "s"(s0_), [s1] "s"(s1_) : "memory", "scc")
#define QKV_PROX(vxd, rX_, m0x_, s0_, s1_) asm volatile( \
"s_add_u32 m0, %[m0x], 0\n" \
"s_nop 0\n" \
"buffer_load_dwordx4 v182, %[rX], %[s0] offen offset:0 sc1 lds\n" \
"buffer_load_dwordx4 v182, %[rX], %[s0] offen offset:1024 sc1 lds\n" \
"s_add_u32 m0, %[m0x], 40960\n" \
"s_nop 0\n" \
"buffer_load_dwordx4 v182, %[rX], %[s1] offen offset:0 sc1 lds\n" \
"buffer_load_dwordx4 v182, %[rX], %[s1] offen offset:1024 sc1 lds\n" \
    :: "{v182}"(vxd[0]), [rX] "s"(rX_), [m0x] "s"(m0x_), [s0] "s"(s0_), [s1] "s"(s1_) : "memory", "scc")
#define QKV_MAIN(acc, vxd, vwd, vxr, vwr, rX_, rW_, sX_, sW_, m0x_, m0w_, cnt_, u_, rot_, tp_) asm volatile( \
"s_waitcnt lgkmcnt(0)\n" \
"s_waitcnt vmcnt(2)\n" \
"s_barrier\n" \
"ds_read_b64_tr_b16 v[80:81], v178 offset:16384\n" \
"ds_read_b64_tr_b16 v[82:83], v178 offset:16896\n" \
"ds_read_b128 v[48:51], v176 offset:0\n" \
"ds_read_b128 v[52:55], v176 offset:256\n" \
"s_add_u32 m0, %[m0x], 81920\n" \
"s_nop 0\n" \
"buffer_load_dwordx4 v182, %[rX], %[sX] offen offset:0 sc1 lds\n" \
"ds_read_b64_tr_b16 v[84:85], v180 offset:16384\n" \
"ds_read_b64_tr_b16 v[86:87], v180 offset:16896\n" \
"ds_read_b64_tr_b16 v[88:89], v178 offset:24576\n" \
"ds_read_b64_tr_b16 v[90:91], v178 offset:25088\n" \
"ds_read_b64_tr_b16 v[92:93], v180 offset:24576\n" \
"ds_read_b64_tr_b16 v[94:95], v180 offset:25088\n" \
"ds_read_b64_tr_b16 v[96:97], v178 offset:32768\n" \
"buffer_load_dwordx4 v182, %[rX], %[sX] offen offset:1024 sc1 lds\n" \
"ds_read_b64_tr_b16 v[98:99], v178 offset:33280\n" \
"ds_read_b64_tr_b16 v[100:101], v180 offset:32768\n" \
"ds_read_b64_tr_b16 v[102:103], v180 offset:33280\n" \
"ds_read_b64_tr_b16 v[104:105], v178 offset:20480\n" \
"ds_read_b64_tr_b16 v[106:107], v178 offset:20992\n" \
"ds_read_b128 v[56:59], v176 offset:2048\n" \
"ds_read_b128 v[60:63], v176 offset:2304\n" \
"s_add_u32 m0, %[m0w], 98304\n" \
"s_nop 0\n" \
"buffer_load_dwordx4 v183, %[rW], %[sW] offen offset:0 lds\n" \
"ds_read_b64_tr_b16 v[108:109], v180 offset:20480\n" \
"ds_read_b64_tr_b16 v[110:111], v180 offset:20992\n" \
"ds_read_b64_tr_b16 v[112:113], v178 offset:28672\n" \
"ds_read_b64_tr_b16 v[114:115], v178 offset:29184\n" \
"ds_read_b64_tr_b16 v[116:117], v180 offset:28672\n" \
"ds_read_b64_tr_b16 v[118:119], v180 offset:29184\n" \
"ds_read_b64_tr_b16 v[120:121], v178 offset:36864\n" \
"buffer_load_dwordx4 v184, %[rW], %[sW] offen offset:1024 lds\n" \
"ds_read_b64_tr_b16 v[122:123], v178 offset:37376\n" \
"ds_read_b64_tr_b16 v[124:125], v180 offset:36864\n" \
"ds_read_b64_tr_b16 v[126:127], v180 offset:37376\n" \
"buffer_load_dwordx4 v185, %[rW], %[sW] offen offset:2048 lds\n" \
"s_add_u32 %[u], %[u], 1\n" \
"s_add_u32 %[tp], %[u], %[rot]\n" \
"s_and_b32 %[tp], %[tp], 7\n" \
"s_and_b32 %[sX], %[u], 2147483640\n" \
"s_or_b32 %[tp], %[tp], %[sX]\n" \
"s_mul_i32 %[sX], %[tp], 4096\n" \
"s_mul_i32 %[sW], %[tp], 393216\n" \
"s_waitcnt vmcnt(5) lgkmcnt(0)\n" \
"s_barrier\n" \
"v_mfma_f32_16x16x32_bf16 v[0:3], v[80:83], v[48:51], 0\n" \
"ds_read_b64_tr_b16 v[128:129], v178 offset:57344\n" \
"ds_read_b64_tr_b16 v[130:131], v178 offset:57856\n" \
"v_mfma_f32_16x16x32_bf16 v[4:7], v[80:83], v[52:55], 0\n" \
"ds_read_b128 v[64:67], v176 offset:40960\n" \
"v_mfma_f32_16x16x32_bf16 v[8:11], v[84:87], v[48:51], 0\n" \
"ds_read_b128 v[68:71], v176 offset:41216\n" \
"s_add_u32 m0, %[m0x], 0\n" \
"s_nop 0\n" \
"buffer_load_dwordx4 v182, %[rX], %[sX] offen offset:0 sc1 lds\n" \
"ds_read_b64_tr_b16 v[132:133], v180 offset:57344\n" \
"v_mfma_f32_16x16x32_bf16 v[12:15], v[84:87], v[52:55], 0\n" \
"ds_read_b64_tr_b16 v[134:135], v180 offset:57856\n" \
"v_mfma_f32_16x16x32_bf16 v[16:19], v[48:51], v[88:91], 0\n" \
"ds_read_b64_tr_b16 v[136:137], v179 offset:0\n" \
"ds_read_b64_tr_b16 v[138:139], v179 offset:512\n" \
"v_mfma_f32_16x16x32_bf16 v[20:23], v[52:55], v[88:91], 0\n" \
"ds_read_b64_tr_b16 v[140:141], v181 offset:0\n" \
"v_mfma_f32_16x16x32_bf16 v[24:27], v[48:51], v[92:95], 0\n" \
"ds_read_b64_tr_b16 v[142:143], v181 offset:512\n" \
"ds_read_b64_tr_b16 v[144:145], v179 offset:8192\n" \
"v_mfma_f32_16x16x32_bf16 v[28:31], v[52:55], v[92:95], 0\n" \
"buffer_load_dwordx4 v182, %[rX], %[sX] offen offset:1024 sc1 lds\n" \
"ds_read_b64_tr_b16 v[146:147], v179 offset:8704\n" \
"v_mfma_f32_16x16x32_bf16 v[32:35], v[48:51], v[96:99], 0\n" \
"ds_read_b64_tr_b16 v[148:149], v181 offset:8192\n" \
"ds_read_b64_tr_b16 v[150:151], v181 offset:8704\n" \
"v_mfma_f32_16x16x32_bf16 v[36:39], v[52:55], v[96:99], 0\n" \
"ds_read_b64_tr_b16 v[152:153], v178 offset:61440\n" \
"v_mfma_f32_16x16x32_bf16 v[40:43], v[48:51], v[100:103], 0\n" \
"ds_read_b64_tr_b16 v[154:155], v178 offset:61952\n" \
"ds_read_b128 v[72:75], v176 offset:43008\n" \
"v_mfma_f32_16x16x32_bf16 v[44:47], v[52:55], v[100:103], 0\n" \
"ds_read_b128 v[76:79], v176 offset:43264\n" \
"v_mfma_f32_16x16x32_bf16 v[0:3], v[104:107], v[56:59], v[0:3]\n" \
"s_add_u32 m0, %[m0w], 16384\n" \
"s_nop 0\n" \
"buffer_load_dwordx4 v183, %[rW], %[sW] offen offset:0 lds\n" \
"ds_read_b64_tr_b16 v[156:157], v180 offset:61440\n" \
"v_mfma_f32_16x16x32_bf16 v[4:7], v[104:107], v[60:63], v[4:7]\n" \
"ds_read_b64_tr_b16 v[158:159], v180 offset:61952\n" \
"ds_read_b64_tr_b16 v[160:161], v179 offset:4096\n" \
"v_mfma_f32_16x16x32_bf16 v[8:11], v[108:111], v[56:59], v[8:11]\n" \
"ds_read_b64_tr_b16 v[162:163], v179 offset:4608\n" \
"v_mfma_f32_16x16x32_bf16 v[12:15], v[108:111], v[60:63], v[12:15]\n" \
"ds_read_b64_tr_b16 v[164:165], v181 offset:4096\n" \
"ds_read_b64_tr_b16 v[166:167], v181 offset:4608\n" \
"v_mfma_f32_16x16x32_bf16 v[16:19], v[56:59], v[112:115], v[16:19]\n" \
"ds_read_b64_tr_b16 v[168:169], v179 offset:12288\n" \
"buffer_load_dwordx4 v184, %[rW], %[sW] offen offset:1024 lds\n" \
"v_mfma_f32_16x16x32_bf16 v[20:23], v[60:63], v[112:115], v[20:23]\n" \
"ds_read_b64_tr_b16 v[170:171], v179 offset:12800\n" \
"ds_read_b64_tr_b16 v[172:173], v181 offset:12288\n" \
"v_mfma_f32_16x16x32_bf16 v[24:27], v[56:59], v[116:119], v[24:27]\n" \
"ds_read_b64_tr_b16 v[174:175], v181 offset:12800\n" \
"v_mfma_f32_16x16x32_bf16 v[28:31], v[60:63], v[116:119], v[28:31]\n" \
"v_mfma_f32_16x16x32_bf16 v[32:35], v[56:59], v[120:123], v[32:35]\n" \
"v_mfma_f32_16x16x32_bf16 v[36:39], v[60:63], v[120:123], v[36:39]\n" \
"buffer_load_dwordx4 v185, %[rW], %[sW] offen offset:2048 lds\n" \
"s_add_u32 %[u], %[u], 1\n" \
"s_add_u32 %[tp], %[u], %[rot]\n" \
"s_and_b32 %[tp], %[tp], 7\n" \
"s_and_b32 %[sX], %[u], 2147483640\n" \
"s_or_b32 %[tp], %[tp], %[sX]\n" \
"s_mul_i32 %[sX], %[tp], 4096\n" \
"s_mul_i32 %[sW], %[tp], 393216\n" \
"v_mfma_f32_16x16x32_bf16 v[40:43], v[56:59], v[124:127], v[40:43]\n" \
"v_mfma_f32_16x16x32_bf16 v[44:47], v[60:63], v[124:127], v[44:47]\n" \
"s_waitcnt vmcnt(5) lgkmcnt(0)\n" \
"s_barrier\n" \
"s_mov_b32 %[cnt], 2\n" \
"LOOP_%=:\n" \
"v_mfma_f32_16x16x32_bf16 v[0:3], v[128:131], v[64:67], v[0:3]\n" \
"ds_read_b64_tr_b16 v[80:81], v179 offset:32768\n" \
"ds_read_b64_tr_b16 v[82:83], v179 offset:33280\n" \
"v_mfma_f32_16x16x32_bf16 v[4:7], v[128:131], v[68:71], v[4:7]\n" \
"ds_read_b128 v[48:51], v177 offset:16384\n" \
"v_mfma_f32_16x16x32_bf16 v[8:11], v[132:135], v[64:67], v[8:11]\n" \
"ds_read_b128 v[52:55], v177 offset:16640\n" \
"s_add_u32 m0, %[m0x], 40960\n" \
"s_nop 0\n" \
"buffer_load_dwordx4 v182, %[rX], %[sX] offen offset:0 sc1 lds\n" \
"ds_read_b64_tr_b16 v[84:85], v181 offset:32768\n" \
"v_mfma_f32_16x16x32_bf16 v[12:15], v[132:135], v[68:71], v[12:15]\n" \
"ds_read_b64_tr_b16 v[86:87], v181 offset:33280\n" \
"v_mfma_f32_16x16x32_bf16 v[16:19], v[64:67], v[136:139], v[16:19]\n" \
"ds_read_b64_tr_b16 v[88:89], v179 offset:40960\n" \
"ds_read_b64_tr_b16 v[90:91], v179 offset:41472\n" \
"v_mfma_f32_16x16x32_bf16 v[20:23], v[68:71], v[136:139], v[20:23]\n" \
"ds_read_b64_tr_b16 v[92:93], v181 offset:40960\n" \
"v_mfma_f32_16x16x32_bf16 v[24:27], v[64:67], v[140:143], v[24:27]\n" \
"ds_read_b64_tr_b16 v[94:95], v181 offset:41472\n" \
"ds_read_b64_tr_b16 v[96:97], v179 offset:49152\n" \
"v_mfma_f32_16x16x32_bf16 v[28:31], v[68:71], v[140:143], v[28:31]\n" \
"buffer_load_dwordx4 v182, %[rX], %[sX] offen offset:1024 sc1 lds\n" \
"ds_read_b64_tr_b16 v[98:99], v179 offset:49664\n" \
"v_mfma_f32_16x16x32_bf16 v[32:35], v[64:67], v[144:147], v[32:35]\n" \
"ds_read_b64_tr_b16 v[100:101], v181 offset:49152\n" \
"ds_read_b64_tr_b16 v[102:103], v181 offset:49664\n" \
"v_mfma_f32_16x16x32_bf16 v[36:39], v[68:71], v[144:147], v[36:39]\n" \
"ds_read_b64_tr_b16 v[104:105], v179 offset:36864\n" \
"v_mfma_f32_16x16x32_bf16 v[40:43], v[64:67], v[148:151], v[40:43]\n" \
"ds_read_b64_tr_b16 v[106:107], v179 offset:37376\n" \
"ds_read_b128 v[56:59], v177 offset:18432\n" \
"v_mfma_f32_16x16x32_bf16 v[44:47], v[68:71], v[148:151], v[44:47]\n" \
"ds_read_b128 v[60:63], v177 offset:18688\n" \
"v_mfma_f32_16x16x32_bf16 v[0:3], v[152:155], v[72:75], v[0:3]\n" \
"s_add_u32 m0, %[m0w], 57344\n" \
"s_nop 0\n" \
"buffer_load_dwordx4 v183, %[rW], %[sW] offen offset:0 lds\n" \
"ds_read_b64_tr_b16 v[108:109], v181 offset:36864\n" \
"v_mfma_f32_16x16x32_bf16 v[4:7], v[152:155], v[76:79], v[4:7]\n" \
"ds_read_b64_tr_b16 v[110:111], v181 offset:37376\n" \
"ds_read_b64_tr_b16 v[112:113], v179 offset:45056\n" \
"v_mfma_f32_16x16x32_bf16 v[8:11], v[156:159], v[72:75], v[8:11]\n" \
"ds_read_b64_tr_b16 v[114:115], v179 offset:45568\n" \
"v_mfma_f32_16x16x32_bf16 v[12:15], v[156:159], v[76:79], v[12:15]\n" \
"ds_read_b64_tr_b16 v[116:117], v181 offset:45056\n" \
"ds_read_b64_tr_b16 v[118:119], v181 offset:45568\n" \
"v_mfma_f32_16x16x32_bf16 v[16:19], v[72:75], v[160:163], v[16:19]\n" \
"ds_read_b64_tr_b16 v[120:121], v179 offset:53248\n" \
"buffer_load_dwordx4 v184, %[rW], %[sW] offen offset:1024 lds\n" \
"v_mfma_f32_16x16x32_bf16 v[20:23], v[76:79], v[160:163], v[20:23]\n" \
"ds_read_b64_tr_b16 v[122:123], v179 offset:53760\n" \
"ds_read_b64_tr_b16 v[124:125], v181 offset:53248\n" \
"v_mfma_f32_16x16x32_bf16 v[24:27], v[72:75], v[164:167], v[24:27]\n" \
"ds_read_b64_tr_b16 v[126:127], v181 offset:53760\n" \
"v_mfma_f32_16x16x32_bf16 v[28:31], v[76:79], v[164:167], v[28:31]\n" \
"v_mfma_f32_16x16x32_bf16 v[32:35], v[72:75], v[168:171], v[32:35]\n" \
"v_mfma_f32_16x16x32_bf16 v[36:39], v[76:79], v[168:171], v[36:39]\n" \
"buffer_load_dwordx4 v185, %[rW], %[sW] offen offset:2048 lds\n" \
"s_add_u32 %[u], %[u], 1\n" \
"s_add_u32 %[tp], %[u], %[rot]\n" \
"s_and_b32 %[tp], %[tp], 7\n" \
"s_and_b32 %[sX], %[u], 2147483640\n" \
"s_or_b32 %[tp], %[tp], %[sX]\n" \
"s_mul_i32 %[sX], %[tp], 4096\n" \
"s_mul_i32 %[sW], %[tp], 393216\n" \
"v_mfma_f32_16x16x32_bf16 v[40:43], v[72:75], v[172:175], v[40:43]\n" \
"v_mfma_f32_16x16x32_bf16 v[44:47], v[76:79], v[172:175], v[44:47]\n" \
"s_waitcnt vmcnt(5) lgkmcnt(0)\n" \
"s_barrier\n" \
"v_mfma_f32_16x16x32_bf16 v[0:3], v[80:83], v[48:51], v[0:3]\n" \
"ds_read_b64_tr_b16 v[128:129], v178 offset:16384\n" \
"ds_read_b64_tr_b16 v[130:131], v178 offset:16896\n" \
"v_mfma_f32_16x16x32_bf16 v[4:7], v[80:83], v[52:55], v[4:7]\n" \
"ds_read_b128 v[64:67], v176 offset:0\n" \
"v_mfma_f32_16x16x32_bf16 v[8:11], v[84:87], v[48:51], v[8:11]\n" \
"ds_read_b128 v[68:71], v176 offset:256\n" \
"s_add_u32 m0, %[m0x], 81920\n" \
"s_nop 0\n" \
"buffer_load_dwordx4 v182, %[rX], %[sX] offen offset:0 sc1 lds\n" \
"ds_read_b64_tr_b16 v[132:133], v180 offset:16384\n" \
"v_mfma_f32_16x16x32_bf16 v[12:15], v[84:87], v[52:55], v[12:15]\n" \
"ds_read_b64_tr_b16 v[134:135], v180 offset:16896\n" \
"v_mfma_f32_16x16x32_bf16 v[16:19], v[48:51], v[88:91], v[16:19]\n" \
"ds_read_b64_tr_b16 v[136:137], v178 offset:24576\n" \
"ds_read_b64_tr_b16 v[138:139], v178 offset:25088\n" \
"v_mfma_f32_16x16x32_bf16 v[20:23], v[52:55], v[88:91], v[20:23]\n" \
"ds_read_b64_tr_b16 v[140:141], v180 offset:24576\n" \
"v_mfma_f32_16x16x32_bf16 v[24:27], v[48:51], v[92:95], v[24:27]\n" \
"ds_read_b64_tr_b16 v[142:143], v180 offset:25088\n" \
"ds_read_b64_tr_b16 v[144:145], v178 offset:32768\n" \
"v_mfma_f32_16x16x32_bf16 v[28:31], v[52:55], v[92:95], v[28:31]\n" \
"buffer_load_dwordx4 v182, %[rX], %[sX] offen offset:1024 sc1 lds\n" \
"ds_read_b64_tr_b16 v[146:147], v178 offset:33280\n" \
"v_mfma_f32_16x16x32_bf16 v[32:35], v[48:51], v[96:99], v[32:35]\n" \
"ds_read_b64_tr_b16 v[148:149], v180 offset:32768\n" \
"ds_read_b64_tr_b16 v[150:151], v180 offset:33280\n" \
"v_mfma_f32_16x16x32_bf16 v[36:39], v[52:55], v[96:99], v[36:39]\n" \
"ds_read_b64_tr_b16 v[152:153], v178 offset:20480\n" \
"v_mfma_f32_16x16x32_bf16 v[40:43], v[48:51], v[100:103], v[40:43]\n" \
"ds_read_b64_tr_b16 v[154:155], v178 offset:20992\n" \
"ds_read_b128 v[72:75], v176 offset:2048\n" \
"v_mfma_f32_16x16x32_bf16 v[44:47], v[52:55], v[100:103], v[44:47]\n" \
"ds_read_b128 v[76:79], v176 offset:2304\n" \
"v_mfma_f32_16x16x32_bf16 v[0:3], v[104:107], v[56:59], v[0:3]\n" \
"s_add_u32 m0, %[m0w], 98304\n" \
"s_nop 0\n" \
"buffer_load_dwordx4 v183, %[rW], %[sW] offen offset:0 lds\n" \
"ds_read_b64_tr_b16 v[156:157], v180 offset:20480\n" \
"v_mfma_f32_16x16x32_bf16 v[4:7], v[104:107], v[60:63], v[4:7]\n" \
"ds_read_b64_tr_b16 v[158:159], v180 offset:20992\n" \
"ds_read_b64_tr_b16 v[160:161], v178 offset:28672\n" \
"v_mfma_f32_16x16x32_bf16 v[8:11], v[108:111], v[56:59], v[8:11]\n" \
"ds_read_b64_tr_b16 v[162:163], v178 offset:29184\n" \
"v_mfma_f32_16x16x32_bf16 v[12:15], v[108:111], v[60:63], v[12:15]\n" \
"ds_read_b64_tr_b16 v[164:165], v180 offset:28672\n" \
"ds_read_b64_tr_b16 v[166:167], v180 offset:29184\n" \
"v_mfma_f32_16x16x32_bf16 v[16:19], v[56:59], v[112:115], v[16:19]\n" \
"ds_read_b64_tr_b16 v[168:169], v178 offset:36864\n" \
"buffer_load_dwordx4 v184, %[rW], %[sW] offen offset:1024 lds\n" \
"v_mfma_f32_16x16x32_bf16 v[20:23], v[60:63], v[112:115], v[20:23]\n" \
"ds_read_b64_tr_b16 v[170:171], v178 offset:37376\n" \
"ds_read_b64_tr_b16 v[172:173], v180 offset:36864\n" \
"v_mfma_f32_16x16x32_bf16 v[24:27], v[56:59], v[116:119], v[24:27]\n" \
"ds_read_b64_tr_b16 v[174:175], v180 offset:37376\n" \
"v_mfma_f32_16x16x32_bf16 v[28:31], v[60:63], v[116:119], v[28:31]\n" \
"v_mfma_f32_16x16x32_bf16 v[32:35], v[56:59], v[120:123], v[32:35]\n" \
"v_mfma_f32_16x16x32_bf16 v[36:39], v[60:63], v[120:123], v[36:39]\n" \
"buffer_load_dwordx4 v185, %[rW], %[sW] offen offset:2048 lds\n" \
"s_add_u32 %[u], %[u], 1\n" \
"s_add_u32 %[tp], %[u], %[rot]\n" \
"s_and_b32 %[tp], %[tp], 7\n" \
"s_and_b32 %[sX], %[u], 2147483640\n" \
"s_or_b32 %[tp], %[tp], %[sX]\n" \
"s_mul_i32 %[sX], %[tp], 4096\n" \
"s_mul_i32 %[sW], %[tp], 393216\n" \
"v_mfma_f32_16x16x32_bf16 v[40:43], v[56:59], v[124:127], v[40:43]\n" \
"v_mfma_f32_16x16x32_bf16 v[44:47], v[60:63], v[124:127], v[44:47]\n" \
"s_waitcnt vmcnt(5) lgkmcnt(0)\n" \
"s_barrier\n" \
"v_mfma_f32_16x16x32_bf16 v[0:3], v[128:131], v[64:67], v[0:3]\n" \
"ds_read_b64_tr_b16 v[80:81], v178 offset:57344\n" \
"ds_read_b64_tr_b16 v[82:83], v178 offset:57856\n" \
"v_mfma_f32_16x16x32_bf16 v[4:7], v[128:131], v[68:71], v[4:7]\n" \
"ds_read_b128 v[48:51], v176 offset:40960\n" \
"v_mfma_f32_16x16x32_bf16 v[8:11], v[132:135], v[64:67], v[8:11]\n" \
"ds_read_b128 v[52:55], v176 offset:41216\n" \
"s_add_u32 m0, %[m0x], 0\n" \
"s_nop 0\n" \
"buffer_load_dwordx4 v182, %[rX], %[sX] offen offset:0 sc1 lds\n" \
"ds_read_b64_tr_b16 v[84:85], v180 offset:57344\n" \
"v_mfma_f32_16x16x32_bf16 v[12:15], v[132:135], v[68:71], v[12:15]\n" \
"ds_read_b64_tr_b16 v[86:87], v180 offset:57856\n" \
"v_mfma_f32_16x16x32_bf16 v[16:19], v[64:67], v[136:139], v[16:19]\n" \
"ds_read_b64_tr_b16 v[88:89], v179 offset:0\n" \
"ds_read_b64_tr_b16 v[90:91], v179 offset:512\n" \
"v_mfma_f32_16x16x32_bf16 v[20:23], v[68:71], v[136:139], v[20:23]\n" \
"ds_read_b64_tr_b16 v[92:93], v181 offset:0\n" \
"v_mfma_f32_16x16x32_bf16 v[24:27], v[64:67], v[140:143], v[24:27]\n" \
"ds_read_b64_tr_b16 v[94:95], v181 offset:512\n" \
"ds_read_b64_tr_b16 v[96:97], v179 offset:8192\n" \
"v_mfma_f32_16x16x32_bf16 v[28:31], v[68:71], v[140:143], v[28:31]\n" \
"buffer_load_dwordx4 v182, %[rX], %[sX] offen offset:1024 sc1 lds\n" \
"ds_read_b64_tr_b16 v[98:99], v179 offset:8704\n" \
"v_mfma_f32_16x16x32_bf16 v[32:35], v[64:67], v[144:147], v[32:35]\n" \
"ds_read_b64_tr_b16 v[100:101], v181 offset:8192\n" \
"ds_read_b64_tr_b16 v[102:103], v181 offset:8704\n" \
"v_mfma_f32_16x16x32_bf16 v[36:39], v[68:71], v[144:147], v[36:39]\n" \
"ds_read_b64_tr_b16 v[104:105], v178 offset:61440\n" \
"v_mfma_f32_16x16x32_bf16 v[40:43], v[64:67], v[148:151], v[40:43]\n" \
"ds_read_b64_tr_b16 v[106:107], v178 offset:61952\n" \
"ds_read_b128 v[56:59], v176 offset:43008\n" \
"v_mfma_f32_16x16x32_bf16 v[44:47], v[68:71], v[148:151], v[44:47]\n" \
"ds_read_b128 v[60:63], v176 offset:43264\n" \
"v_mfma_f32_16x16x32_bf16 v[0:3], v[152:155], v[72:75], v[0:3]\n" \
"s_add_u32 m0, %[m0w], 16384\n" \
"s_nop 0\n" \
"buffer_load_dwordx4 v183, %[rW], %[sW] offen offset:0 lds\n" \
"ds_read_b64_tr_b16 v[108:109], v180 offset:61440\n" \
"v_mfma_f32_16x16x32_bf16 v[4:7], v[152:155], v[76:79], v[4:7]\n" \
"ds_read_b64_tr_b16 v[110:111], v180 offset:61952\n" \
"ds_read_b64_tr_b16 v[112:113], v179 offset:4096\n" \
"v_mfma_f32_16x16x32_bf16 v[8:11], v[156:159], v[72:75], v[8:11]\n" \
"ds_read_b64_tr_b16 v[114:115], v179 offset:4608\n" \
"v_mfma_f32_16x16x32_bf16 v[12:15], v[156:159], v[76:79], v[12:15]\n" \
"ds_read_b64_tr_b16 v[116:117], v181 offset:4096\n" \
"ds_read_b64_tr_b16 v[118:119], v181 offset:4608\n" \
"v_mfma_f32_16x16x32_bf16 v[16:19], v[72:75], v[160:163], v[16:19]\n" \
"ds_read_b64_tr_b16 v[120:121], v179 offset:12288\n" \
"buffer_load_dwordx4 v184, %[rW], %[sW] offen offset:1024 lds\n" \
"v_mfma_f32_16x16x32_bf16 v[20:23], v[76:79], v[160:163], v[20:23]\n" \
"ds_read_b64_tr_b16 v[122:123], v179 offset:12800\n" \
"ds_read_b64_tr_b16 v[124:125], v181 offset:12288\n" \
"v_mfma_f32_16x16x32_bf16 v[24:27], v[72:75], v[164:167], v[24:27]\n" \
"ds_read_b64_tr_b16 v[126:127], v181 offset:12800\n" \
"v_mfma_f32_16x16x32_bf16 v[28:31], v[76:79], v[164:167], v[28:31]\n" \
"v_mfma_f32_16x16x32_bf16 v[32:35], v[72:75], v[168:171], v[32:35]\n" \
"v_mfma_f32_16x16x32_bf16 v[36:39], v[76:79], v[168:171], v[36:39]\n" \
"buffer_load_dwordx4 v185, %[rW], %[sW] offen offset:2048 lds\n" \
"s_add_u32 %[u], %[u], 1\n" \
"s_add_u32 %[tp], %[u], %[rot]\n" \
"s_and_b32 %[tp], %[tp], 7\n" \
"s_and_b32 %[sX], %[u], 2147483640\n" \
"s_or_b32 %[tp], %[tp], %[sX]\n" \
"s_mul_i32 %[sX], %[tp], 4096\n" \
"s_mul_i32 %[sW], %[tp], 393216\n" \
"v_mfma_f32_16x16x32_bf16 v[40:43], v[72:75], v[172:175], v[40:43]\n" \
"v_mfma_f32_16x16x32_bf16 v[44:47], v[76:79], v[172:175], v[44:47]\n" \
"s_waitcnt vmcnt(5) lgkmcnt(0)\n" \
"s_barrier\n" \
"v_mfma_f32_16x16x32_bf16 v[0:3], v[80:83], v[48:51], v[0:3]\n" \
"ds_read_b64_tr_b16 v[128:129], v179 offset:32768\n" \
"ds_read_b64_tr_b16 v[130:131], v179 offset:33280\n" \
"v_mfma_f32_16x16x32_bf16 v[4:7], v[80:83], v[52:55], v[4:7]\n" \
"ds_read_b128 v[64:67], v177 offset:16384\n" \
"v_mfma_f32_16x16x32_bf16 v[8:11], v[84:87], v[48:51], v[8:11]\n" \
"ds_read_b128 v[68:71], v177 offset:16640\n" \
"s_add_u32 m0, %[m0x], 40960\n" \
"s_nop 0\n" \
"buffer_load_dwordx4 v182, %[rX], %[sX] offen offset:0 sc1 lds\n" \
"ds_read_b64_tr_b16 v[132:133], v181 offset:32768\n" \
"v_mfma_f32_16x16x32_bf16 v[12:15], v[84:87], v[52:55], v[12:15]\n" \
"ds_read_b64_tr_b16 v[134:135], v181 offset:33280\n" \
"v_mfma_f32_16x16x32_bf16 v[16:19], v[48:51], v[88:91], v[16:19]\n" \
"ds_read_b64_tr_b16 v[136:137], v179 offset:40960\n" \
"ds_read_b64_tr_b16 v[138:139], v179 offset:41472\n" \
"v_mfma_f32_16x16x32_bf16 v[20:23], v[52:55], v[88:91], v[20:23]\n" \
"ds_read_b64_tr_b16 v[140:141], v181 offset:40960\n" \
"v_mfma_f32_16x16x32_bf16 v[24:27], v[48:51], v[92:95], v[24:27]\n" \
"ds_read_b64_tr_b16 v[142:143], v181 offset:41472\n" \
"ds_read_b64_tr_b16 v[144:145], v179 offset:49152\n" \
"v_mfma_f32_16x16x32_bf16 v[28:31], v[52:55], v[92:95], v[28:31]\n" \
"buffer_load_dwordx4 v182, %[rX], %[sX] offen offset:1024 sc1 lds\n" \
"ds_read_b64_tr_b16 v[146:147], v179 offset:49664\n" \
"v_mfma_f32_16x16x32_bf16 v[32:35], v[48:51], v[96:99], v[32:35]\n" \
"ds_read_b64_tr_b16 v[148:149], v181 offset:49152\n" \
"ds_read_b64_tr_b16 v[150:151], v181 offset:49664\n" \
"v_mfma_f32_16x16x32_bf16 v[36:39], v[52:55], v[96:99], v[36:39]\n" \
"ds_read_b64_tr_b16 v[152:153], v179 offset:36864\n" \
"v_mfma_f32_16x16x32_bf16 v[40:43], v[48:51], v[100:103], v[40:43]\n" \
"ds_read_b64_tr_b16 v[154:155], v179 offset:37376\n" \
"ds_read_b128 v[72:75], v177 offset:18432\n" \
"v_mfma_f32_16x16x32_bf16 v[44:47], v[52:55], v[100:103], v[44:47]\n" \
"ds_read_b128 v[76:79], v177 offset:18688\n" \
"v_mfma_f32_16x16x32_bf16 v[0:3], v[104:107], v[56:59], v[0:3]\n" \
"s_add_u32 m0, %[m0w], 57344\n" \
"s_nop 0\n" \
"buffer_load_dwordx4 v183, %[rW], %[sW] offen offset:0 lds\n" \
"ds_read_b64_tr_b16 v[156:157], v181 offset:36864\n" \
"v_mfma_f32_16x16x32_bf16 v[4:7], v[104:107], v[60:63], v[4:7]\n" \
"ds_read_b64_tr_b16 v[158:159], v181 offset:37376\n" \
"ds_read_b64_tr_b16 v[160:161], v179 offset:45056\n" \
"v_mfma_f32_16x16x32_bf16 v[8:11], v[108:111], v[56:59], v[8:11]\n" \
"ds_read_b64_tr_b16 v[162:163], v179 offset:45568\n" \
"v_mfma_f32_16x16x32_bf16 v[12:15], v[108:111], v[60:63], v[12:15]\n" \
"ds_read_b64_tr_b16 v[164:165], v181 offset:45056\n" \
"ds_read_b64_tr_b16 v[166:167], v181 offset:45568\n" \
"v_mfma_f32_16x16x32_bf16 v[16:19], v[56:59], v[112:115], v[16:19]\n" \
"ds_read_b64_tr_b16 v[168:169], v179 offset:53248\n" \
"buffer_load_dwordx4 v184, %[rW], %[sW] offen offset:1024 lds\n" \
"v_mfma_f32_16x16x32_bf16 v[20:23], v[60:63], v[112:115], v[20:23]\n" \
"ds_read_b64_tr_b16 v[170:171], v179 offset:53760\n" \
"ds_read_b64_tr_b16 v[172:173], v181 offset:53248\n" \
"v_mfma_f32_16x16x32_bf16 v[24:27], v[56:59], v[116:119], v[24:27]\n" \
"ds_read_b64_tr_b16 v[174:175], v181 offset:53760\n" \
"v_mfma_f32_16x16x32_bf16 v[28:31], v[60:63], v[116:119], v[28:31]\n" \
"v_mfma_f32_16x16x32_bf16 v[32:35], v[56:59], v[120:123], v[32:35]\n" \
"v_mfma_f32_16x16x32_bf16 v[36:39], v[60:63], v[120:123], v[36:39]\n" \
"buffer_load_dwordx4 v185, %[rW], %[sW] offen offset:2048 lds\n" \
"s_add_u32 %[u], %[u], 1\n" \
"s_add_u32 %[tp], %[u], %[rot]\n" \
"s_and_b32 %[tp], %[tp], 7\n" \
"s_and_b32 %[sX], %[u], 2147483640\n" \
"s_or_b32 %[tp], %[tp], %[sX]\n" \
"s_mul_i32 %[sX], %[tp], 4096\n" \
"s_mul_i32 %[sW], %[tp], 393216\n" \
"v_mfma_f32_16x16x32_bf16 v[40:43], v[56:59], v[124:127], v[40:43]\n" \
"v_mfma_f32_16x16x32_bf16 v[44:47], v[60:63], v[124:127], v[44:47]\n" \
"s_waitcnt vmcnt(5) lgkmcnt(0)\n" \
"s_barrier\n" \
"v_mfma_f32_16x16x32_bf16 v[0:3], v[128:131], v[64:67], v[0:3]\n" \
"ds_read_b64_tr_b16 v[80:81], v178 offset:16384\n" \
"ds_read_b64_tr_b16 v[82:83], v178 offset:16896\n" \
"v_mfma_f32_16x16x32_bf16 v[4:7], v[128:131], v[68:71], v[4:7]\n" \
"ds_read_b128 v[48:51], v176 offset:0\n" \
"v_mfma_f32_16x16x32_bf16 v[8:11], v[132:135], v[64:67], v[8:11]\n" \
"ds_read_b128 v[52:55], v176 offset:256\n" \
"s_add_u32 m0, %[m0x], 81920\n" \
"s_nop 0\n" \
"buffer_load_dwordx4 v182, %[rX], %[sX] offen offset:0 sc1 lds\n" \
"ds_read_b64_tr_b16 v[84:85], v180 offset:16384\n" \
"v_mfma_f32_16x16x32_bf16 v[12:15], v[132:135], v[68:71], v[12:15]\n" \
"ds_read_b64_tr_b16 v[86:87], v180 offset:16896\n" \
"v_mfma_f32_16x16x32_bf16 v[16:19], v[64:67], v[136:139], v[16:19]\n" \
"ds_read_b64_tr_b16 v[88:89], v178 offset:24576\n" \
"ds_read_b64_tr_b16 v[90:91], v178 offset:25088\n" \
"v_mfma_f32_16x16x32_bf16 v[20:23], v[68:71], v[136:139], v[20:23]\n" \
"ds_read_b64_tr_b16 v[92:93], v180 offset:24576\n" \
"v_mfma_f32_16x16x32_bf16 v[24:27], v[64:67], v[140:143], v[24:27]\n" \
"ds_read_b64_tr_b16 v[94:95], v180 offset:25088\n" \
"ds_read_b64_tr_b16 v[96:97], v178 offset:32768\n" \
"v_mfma_f32_16x16x32_bf16 v[28:31], v[68:71], v[140:143], v[28:31]\n" \
"buffer_load_dwordx4 v182, %[rX], %[sX] offen offset:1024 sc1 lds\n" \
"ds_read_b64_tr_b16 v[98:99], v178 offset:33280\n" \
"v_mfma_f32_16x16x32_bf16 v[32:35], v[64:67], v[144:147], v[32:35]\n" \
"ds_read_b64_tr_b16 v[100:101], v180 offset:32768\n" \
"ds_read_b64_tr_b16 v[102:103], v180 offset:33280\n" \
"v_mfma_f32_16x16x32_bf16 v[36:39], v[68:71], v[144:147], v[36:39]\n" \
"ds_read_b64_tr_b16 v[104:105], v178 offset:20480\n" \
"v_mfma_f32_16x16x32_bf16 v[40:43], v[64:67], v[148:151], v[40:43]\n" \
"ds_read_b64_tr_b16 v[106:107], v178 offset:20992\n" \
"ds_read_b128 v[56:59], v176 offset:2048\n" \
"v_mfma_f32_16x16x32_bf16 v[44:47], v[68:71], v[148:151], v[44:47]\n" \
"ds_read_b128 v[60:63], v176 offset:2304\n" \
"v_mfma_f32_16x16x32_bf16 v[0:3], v[152:155], v[72:75], v[0:3]\n" \
"s_add_u32 m0, %[m0w], 98304\n" \
"s_nop 0\n" \
"buffer_load_dwordx4 v183, %[rW], %[sW] offen offset:0 lds\n" \
"ds_read_b64_tr_b16 v[108:109], v180 offset:20480\n" \
"v_mfma_f32_16x16x32_bf16 v[4:7], v[152:155], v[76:79], v[4:7]\n" \
"ds_read_b64_tr_b16 v[110:111], v180 offset:20992\n" \
"ds_read_b64_tr_b16 v[112:113], v178 offset:28672\n" \
"v_mfma_f32_16x16x32_bf16 v[8:11], v[156:159], v[72:75], v[8:11]\n" \
"ds_read_b64_tr_b16 v[114:115], v178 offset:29184\n" \
"v_mfma_f32_16x16x32_bf16 v[12:15], v[156:159], v[76:79], v[12:15]\n" \
"ds_read_b64_tr_b16 v[116:117], v180 offset:28672\n" \
"ds_read_b64_tr_b16 v[118:119], v180 offset:29184\n" \
"v_mfma_f32_16x16x32_bf16 v[16:19], v[72:75], v[160:163], v[16:19]\n" \
"ds_read_b64_tr_b16 v[120:121], v178 offset:36864\n" \
"buffer_load_dwordx4 v184, %[rW], %[sW] offen offset:1024 lds\n" \
"v_mfma_f32_16x16x32_bf16 v[20:23], v[76:79], v[160:163], v[20:23]\n" \
"ds_read_b64_tr_b16 v[122:123], v178 offset:37376\n" \
"ds_read_b64_tr_b16 v[124:125], v180 offset:36864\n" \
"v_mfma_f32_16x16x32_bf16 v[24:27], v[72:75], v[164:167], v[24:27]\n" \
"ds_read_b64_tr_b16 v[126:127], v180 offset:37376\n" \
"v_mfma_f32_16x16x32_bf16 v[28:31], v[76:79], v[164:167], v[28:31]\n" \
"v_mfma_f32_16x16x32_bf16 v[32:35], v[72:75], v[168:171], v[32:35]\n" \
"v_mfma_f32_16x16x32_bf16 v[36:39], v[76:79], v[168:171], v[36:39]\n" \
"buffer_load_dwordx4 v185, %[rW], %[sW] offen offset:2048 lds\n" \
"s_add_u32 %[u], %[u], 1\n" \
"s_add_u32 %[tp], %[u], %[rot]\n" \
"s_and_b32 %[tp], %[tp], 7\n" \
"s_and_b32 %[sX], %[u], 2147483640\n" \
"s_or_b32 %[tp], %[tp], %[sX]\n" \
"s_mul_i32 %[sX], %[tp], 4096\n" \
"s_mul_i32 %[sW], %[tp], 393216\n" \
"v_mfma_f32_16x16x32_bf16 v[40:43], v[72:75], v[172:175], v[40:43]\n" \
"v_mfma_f32_16x16x32_bf16 v[44:47], v[76:79], v[172:175], v[44:47]\n" \
"s_waitcnt vmcnt(5) lgkmcnt(0)\n" \
"s_barrier\n" \
"v_mfma_f32_16x16x32_bf16 v[0:3], v[80:83], v[48:51], v[0:3]\n" \
"ds_read_b64_tr_b16 v[128:129], v178 offset:57344\n" \
"ds_read_b64_tr_b16 v[130:131], v178 offset:57856\n" \
"v_mfma_f32_16x16x32_bf16 v[4:7], v[80:83], v[52:55], v[4:7]\n" \
"ds_read_b128 v[64:67], v176 offset:40960\n" \
"v_mfma_f32_16x16x32_bf16 v[8:11], v[84:87], v[48:51], v[8:11]\n" \
"ds_read_b128 v[68:71], v176 offset:41216\n" \
"s_add_u32 m0, %[m0x], 0\n" \
"s_nop 0\n" \
"buffer_load_dwordx4 v182, %[rX], %[sX] offen offset:0 sc1 lds\n" \
"ds_read_b64_tr_b16 v[132:133], v180 offset:57344\n" \
"v_mfma_f32_16x16x32_bf16 v[12:15], v[84:87], v[52:55], v[12:15]\n" \
"ds_read_b64_tr_b16 v[134:135], v180 offset:57856\n" \
"v_mfma_f32_16x16x32_bf16 v[16:19], v[48:51], v[88:91], v[16:19]\n" \
"ds_read_b64_tr_b16 v[136:137], v179 offset:0\n" \
"ds_read_b64_tr_b16 v[138:139], v179 offset:512\n" \
"v_mfma_f32_16x16x32_bf16 v[20:23], v[52:55], v[88:91], v[20:23]\n" \
"ds_read_b64_tr_b16 v[140:141], v181 offset:0\n" \
"v_mfma_f32_16x16x32_bf16 v[24:27], v[48:51], v[92:95], v[24:27]\n" \
"ds_read_b64_tr_b16 v[142:143], v181 offset:512\n" \
"ds_read_b64_tr_b16 v[144:145], v179 offset:8192\n" \
"v_mfma_f32_16x16x32_bf16 v[28:31], v[52:55], v[92:95], v[28:31]\n" \
"buffer_load_dwordx4 v182, %[rX], %[sX] offen offset:1024 sc1 lds\n" \
"ds_read_b64_tr_b16 v[146:147], v179 offset:8704\n" \
"v_mfma_f32_16x16x32_bf16 v[32:35], v[48:51], v[96:99], v[32:35]\n" \
"ds_read_b64_tr_b16 v[148:149], v181 offset:8192\n" \
"ds_read_b64_tr_b16 v[150:151], v181 offset:8704\n" \
"v_mfma_f32_16x16x32_bf16 v[36:39], v[52:55], v[96:99], v[36:39]\n" \
"ds_read_b64_tr_b16 v[152:153], v178 offset:61440\n" \
"v_mfma_f32_16x16x32_bf16 v[40:43], v[48:51], v[100:103], v[40:43]\n" \
"ds_read_b64_tr_b16 v[154:155], v178 offset:61952\n" \
"ds_read_b128 v[72:75], v176 offset:43008\n" \
"v_mfma_f32_16x16x32_bf16 v[44:47], v[52:55], v[100:103], v[44:47]\n" \
"ds_read_b128 v[76:79], v176 offset:43264\n" \
"v_mfma_f32_16x16x32_bf16 v[0:3], v[104:107], v[56:59], v[0:3]\n" \
"s_add_u32 m0, %[m0w], 16384\n" \
"s_nop 0\n" \
"buffer_load_dwordx4 v183, %[rW], %[sW] offen offset:0 lds\n" \
"ds_read_b64_tr_b16 v[156:157], v180 offset:61440\n" \
"v_mfma_f32_16x16x32_bf16 v[4:7], v[104:107], v[60:63], v[4:7]\n" \
"ds_read_b64_tr_b16 v[158:159], v180 offset:61952\n" \
"ds_read_b64_tr_b16 v[160:161], v179 offset:4096\n" \
"v_mfma_f32_16x16x32_bf16 v[8:11], v[108:111], v[56:59], v[8:11]\n" \
"ds_read_b64_tr_b16 v[162:163], v179 offset:4608\n" \
"v_mfma_f32_16x16x32_bf16 v[12:15], v[108:111], v[60:63], v[12:15]\n" \
"ds_read_b64_tr_b16 v[164:165], v181 offset:4096\n" \
"ds_read_b64_tr_b16 v[166:167], v181 offset:4608\n" \
"v_mfma_f32_16x16x32_bf16 v[16:19], v[56:59], v[112:115], v[16:19]\n" \
"ds_read_b64_tr_b16 v[168:169], v179 offset:12288\n" \
"buffer_load_dwordx4 v184, %[rW], %[sW] offen offset:1024 lds\n" \
"v_mfma_f32_16x16x32_bf16 v[20:23], v[60:63], v[112:115], v[20:23]\n" \
"ds_read_b64_tr_b16 v[170:171], v179 offset:12800\n" \
"ds_read_b64_tr_b16 v[172:173], v181 offset:12288\n" \
"v_mfma_f32_16x16x32_bf16 v[24:27], v[56:59], v[116:119], v[24:27]\n" \
"ds_read_b64_tr_b16 v[174:175], v181 offset:12800\n" \
"v_mfma_f32_16x16x32_bf16 v[28:31], v[60:63], v[116:119], v[28:31]\n" \
"v_mfma_f32_16x16x32_bf16 v[32:35], v[56:59], v[120:123], v[32:35]\n" \
"v_mfma_f32_16x16x32_bf16 v[36:39], v[60:63], v[120:123], v[36:39]\n" \
"buffer_load_dwordx4 v185, %[rW], %[sW] offen offset:2048 lds\n" \
"s_add_u32 %[u], %[u], 1\n" \
"s_add_u32 %[tp], %[u], %[rot]\n" \
"s_and_b32 %[tp], %[tp], 7\n" \
"s_and_b32 %[sX], %[u], 2147483640\n" \
"s_or_b32 %[tp], %[tp], %[sX]\n" \
"s_mul_i32 %[sX], %[tp], 4096\n" \
"s_mul_i32 %[sW], %[tp], 393216\n" \
"v_mfma_f32_16x16x32_bf16 v[40:43], v[56:59], v[124:127], v[40:43]\n" \
"v_mfma_f32_16x16x32_bf16 v[44:47], v[60:63], v[124:127], v[44:47]\n" \
"s_waitcnt vmcnt(5) lgkmcnt(0)\n" \
"s_barrier\n" \
"s_sub_u32 %[cnt], %[cnt], 1\n" \
"s_cmp_lg_u32 %[cnt], 0\n" \
"s_cbranch_scc1 LOOP_%=\n" \
"v_mfma_f32_16x16x32_bf16 v[0:3], v[128:131], v[64:67], v[0:3]\n" \
"ds_read_b64_tr_b16 v[80:81], v179 offset:32768\n" \
"ds_read_b64_tr_b16 v[82:83], v179 offset:33280\n" \
"v_mfma_f32_16x16x32_bf16 v[4:7], v[128:131], v[68:71], v[4:7]\n" \
"ds_read_b128 v[48:51], v177 offset:16384\n" \
"v_mfma_f32_16x16x32_bf16 v[8:11], v[132:135], v[64:67], v[8:11]\n" \
"ds_read_b128 v[52:55], v177 offset:16640\n" \
"ds_read_b64_tr_b16 v[84:85], v181 offset:32768\n" \
"v_mfma_f32_16x16x32_bf16 v[12:15], v[132:135], v[68:71], v[12:15]\n" \
"ds_read_b64_tr_b16 v[86:87], v181 offset:33280\n" \
"v_mfma_f32_16x16x32_bf16 v[16:19], v[64:67], v[136:139], v[16:19]\n" \
"ds_read_b64_tr_b16 v[88:89], v179 offset:40960\n" \
"ds_read_b64_tr_b16 v[90:91], v179 offset:41472\n" \
"v_mfma_f32_16x16x32_bf16 v[20:23], v[68:71], v[136:139], v[20:23]\n" \
"ds_read_b64_tr_b16 v[92:93], v181 offset:40960\n" \
"v_mfma_f32_16x16x32_bf16 v[24:27], v[64:67], v[140:143], v[24:27]\n" \
"ds_read_b64_tr_b16 v[94:95], v181 offset:41472\n" \
"ds_read_b64_tr_b16 v[96:97], v179 offset:49152\n" \
"v_mfma_f32_16x16x32_bf16 v[28:31], v[68:71], v[140:143], v[28:31]\n" \
"ds_read_b64_tr_b16 v[98:99], v179 offset:49664\n" \
"v_mfma_f32_16x16x32_bf16 v[32:35], v[64:67], v[144:147], v[32:35]\n" \
"ds_read_b64_tr_b16 v[100:101], v181 offset:49152\n" \
"ds_read_b64_tr_b16 v[102:103], v181 offset:49664\n" \
"v_mfma_f32_16x16x32_bf16 v[36:39], v[68:71], v[144:147], v[36:39]\n" \
"ds_read_b64_tr_b16 v[104:105], v179 offset:36864\n" \
"v_mfma_f32_16x16x32_bf16 v[40:43], v[64:67], v[148:151], v[40:43]\n" \
"ds_read_b64_tr_b16 v[106:107], v179 offset:37376\n" \
"ds_read_b128 v[56:59], v177 offset:18432\n" \
"v_mfma_f32_16x16x32_bf16 v[44:47], v[68:71], v[148:151], v[44:47]\n" \
"ds_read_b128 v[60:63], v177 offset:18688\n" \
"v_mfma_f32_16x16x32_bf16 v[0:3], v[152:155], v[72:75], v[0:3]\n" \
"ds_read_b64_tr_b16 v[108:109], v181 offset:36864\n" \
"v_mfma_f32_16x16x32_bf16 v[4:7], v[152:155], v[76:79], v[4:7]\n" \
"ds_read_b64_tr_b16 v[110:111], v181 offset:37376\n" \
"ds_read_b64_tr_b16 v[112:113], v179 offset:45056\n" \
"v_mfma_f32_16x16x32_bf16 v[8:11], v[156:159], v[72:75], v[8:11]\n" \
"ds_read_b64_tr_b16 v[114:115], v179 offset:45568\n" \
"v_mfma_f32_16x16x32_bf16 v[12:15], v[156:159], v[76:79], v[12:15]\n" \
"ds_read_b64_tr_b16 v[116:117], v181 offset:45056\n" \
"ds_read_b64_tr_b16 v[118:119], v181 offset:45568\n" \
"v_mfma_f32_16x16x32_bf16 v[16:19], v[72:75], v[160:163], v[16:19]\n" \
"ds_read_b64_tr_b16 v[120:121], v179 offset:53248\n" \
"v_mfma_f32_16x16x32_bf16 v[20:23], v[76:79], v[160:163], v[20:23]\n" \
"ds_read_b64_tr_b16 v[122:123], v179 offset:53760\n" \
"ds_read_b64_tr_b16 v[124:125], v181 offset:53248\n" \
"v_mfma_f32_16x16x32_bf16 v[24:27], v[72:75], v[164:167], v[24:27]\n" \
"ds_read_b64_tr_b16 v[126:127], v181 offset:53760\n" \
"v_mfma_f32_16x16x32_bf16 v[28:31], v[76:79], v[164:167], v[28:31]\n" \
"v_mfma_f32_16x16x32_bf16 v[32:35], v[72:75], v[168:171], v[32:35]\n" \
"v_mfma_f32_16x16x32_bf16 v[36:39], v[76:79], v[168:171], v[36:39]\n" \
"v_mfma_f32_16x16x32_bf16 v[40:43], v[72:75], v[172:175], v[40:43]\n" \
"v_mfma_f32_16x16x32_bf16 v[44:47], v[76:79], v[172:175], v[44:47]\n" \
"s_waitcnt vmcnt(0) lgkmcnt(0)\n" \
"s_barrier\n" \
"v_mfma_f32_16x16x32_bf16 v[0:3], v[80:83], v[48:51], v[0:3]\n" \
"ds_read_b64_tr_b16 v[128:129], v178 offset:16384\n" \
"ds_read_b64_tr_b16 v[130:131], v178 offset:16896\n" \
"v_mfma_f32_16x16x32_bf16 v[4:7], v[80:83], v[52:55], v[4:7]\n" \
"ds_read_b128 v[64:67], v176 offset:0\n" \
"v_mfma_f32_16x16x32_bf16 v[8:11], v[84:87], v[48:51], v[8:11]\n" \
"ds_read_b128 v[68:71], v176 offset:256\n" \
"ds_read_b64_tr_b16 v[132:133], v180 offset:16384\n" \
"v_mfma_f32_16x16x32_bf16 v[12:15], v[84:87], v[52:55], v[12:15]\n" \
"ds_read_b64_tr_b16 v[134:135], v180 offset:16896\n" \
"v_mfma_f32_16x16x32_bf16 v[16:19], v[48:51], v[88:91], v[16:19]\n" \
"ds_read_b64_tr_b16 v[136:137], v178 offset:24576\n" \
"ds_read_b64_tr_b16 v[138:139], v178 offset:25088\n" \
"v_mfma_f32_16x16x32_bf16 v[20:23], v[52:55], v[88:91], v[20:23]\n" \
"ds_read_b64_tr_b16 v[140:141], v180 offset:24576\n" \
"v_mfma_f32_16x16x32_bf16 v[24:27], v[48:51], v[92:95], v[24:27]\n" \
"ds_read_b64_tr_b16 v[142:143], v180 offset:25088\n" \
"ds_read_b64_tr_b16 v[144:145], v178 offset:32768\n" \
"v_mfma_f32_16x16x32_bf16 v[28:31], v[52:55], v[92:95], v[28:31]\n" \
"ds_read_b64_tr_b16 v[146:147], v178 offset:33280\n" \
"v_mfma_f32_16x16x32_bf16 v[32:35], v[48:51], v[96:99], v[32:35]\n" \
"ds_read_b64_tr_b16 v[148:149], v180 offset:32768\n" \
"ds_read_b64_tr_b16 v[150:151], v180 offset:33280\n" \
"v_mfma_f32_16x16x32_bf16 v[36:39], v[52:55], v[96:99], v[36:39]\n" \
"ds_read_b64_tr_b16 v[152:153], v178 offset:20480\n" \
"v_mfma_f32_16x16x32_bf16 v[40:43], v[48:51], v[100:103], v[40:43]\n" \
"ds_read_b64_tr_b16 v[154:155], v178 offset:20992\n" \
"ds_read_b128 v[72:75], v176 offset:2048\n" \
"v_mfma_f32_16x16x32_bf16 v[44:47], v[52:55], v[100:103], v[44:47]\n" \
"ds_read_b128 v[76:79], v176 offset:2304\n" \
"v_mfma_f32_16x16x32_bf16 v[0:3], v[104:107], v[56:59], v[0:3]\n" \
"ds_read_b64_tr_b16 v[156:157], v180 offset:20480\n" \
"v_mfma_f32_16x16x32_bf16 v[4:7], v[104:107], v[60:63], v[4:7]\n" \
"ds_read_b64_tr_b16 v[158:159], v180 offset:20992\n" \
"ds_read_b64_tr_b16 v[160:161], v178 offset:28672\n" \
"v_mfma_f32_16x16x32_bf16 v[8:11], v[108:111], v[56:59], v[8:11]\n" \
"ds_read_b64_tr_b16 v[162:163], v178 offset:29184\n" \
"v_mfma_f32_16x16x32_bf16 v[12:15], v[108:111], v[60:63], v[12:15]\n" \
"ds_read_b64_tr_b16 v[164:165], v180 offset:28672\n" \
"ds_read_b64_tr_b16 v[166:167], v180 offset:29184\n" \
"v_mfma_f32_16x16x32_bf16 v[16:19], v[56:59], v[112:115], v[16:19]\n" \
"ds_read_b64_tr_b16 v[168:169], v178 offset:36864\n" \
"v_mfma_f32_16x16x32_bf16 v[20:23], v[60:63], v[112:115], v[20:23]\n" \
"ds_read_b64_tr_b16 v[170:171], v178 offset:37376\n" \
"ds_read_b64_tr_b16 v[172:173], v180 offset:36864\n" \
"v_mfma_f32_16x16x32_bf16 v[24:27], v[56:59], v[116:119], v[24:27]\n" \
"ds_read_b64_tr_b16 v[174:175], v180 offset:37376\n" \
"v_mfma_f32_16x16x32_bf16 v[28:31], v[60:63], v[116:119], v[28:31]\n" \
"v_mfma_f32_16x16x32_bf16 v[32:35], v[56:59], v[120:123], v[32:35]\n" \
"v_mfma_f32_16x16x32_bf16 v[36:39], v[60:63], v[120:123], v[36:39]\n" \
"v_mfma_f32_16x16x32_bf16 v[40:43], v[56:59], v[124:127], v[40:43]\n" \
"v_mfma_f32_16x16x32_bf16 v[44:47], v[60:63], v[124:127], v[44:47]\n" \
"s_waitcnt lgkmcnt(15)\n" \
"v_mfma_f32_16x16x32_bf16 v[0:3], v[128:131], v[64:67], v[0:3]\n" \
"v_mfma_f32_16x16x32_bf16 v[4:7], v[128:131], v[68:71], v[4:7]\n" \
"v_mfma_f32_16x16x32_bf16 v[8:11], v[132:135], v[64:67], v[8:11]\n" \
"v_mfma_f32_16x16x32_bf16 v[12:15], v[132:135], v[68:71], v[12:15]\n" \
"v_mfma_f32_16x16x32_bf16 v[16:19], v[64:67], v[136:139], v[16:19]\n" \
"v_mfma_f32_16x16x32_bf16 v[20:23], v[68:71], v[136:139], v[20:23]\n" \
"v_mfma_f32_16x16x32_bf16 v[24:27], v[64:67], v[140:143], v[24:27]\n" \
"v_mfma_f32_16x16x32_bf16 v[28:31], v[68:71], v[140:143], v[28:31]\n" \
"v_mfma_f32_16x16x32_bf16 v[32:35], v[64:67], v[144:147], v[32:35]\n" \
"v_mfma_f32_16x16x32_bf16 v[36:39], v[68:71], v[144:147], v[36:39]\n" \
"s_waitcnt lgkmcnt(14)\n" \
"v_mfma_f32_16x16x32_bf16 v[40:43], v[64:67], v[148:151], v[40:43]\n" \
"v_mfma_f32_16x16x32_bf16 v[44:47], v[68:71], v[148:151], v[44:47]\n" \
"s_waitcnt lgkmcnt(11)\n" \
"v_mfma_f32_16x16x32_bf16 v[0:3], v[152:155], v[72:75], v[0:3]\n" \
"s_waitcnt lgkmcnt(10)\n" \
"v_mfma_f32_16x16x32_bf16 v[4:7], v[152:155], v[76:79], v[4:7]\n" \
"s_waitcnt lgkmcnt(8)\n" \
"v_mfma_f32_16x16x32_bf16 v[8:11], v[156:159], v[72:75], v[8:11]\n" \
"v_mfma_f32_16x16x32_bf16 v[12:15], v[156:159], v[76:79], v[12:15]\n" \
"s_waitcnt lgkmcnt(6)\n" \
"v_mfma_f32_16x16x32_bf16 v[16:19], v[72:75], v[160:163], v[16:19]\n" \
"v_mfma_f32_16x16x32_bf16 v[20:23], v[76:79], v[160:163], v[20:23]\n" \
"s_waitcnt lgkmcnt(4)\n" \
"v_mfma_f32_16x16x32_bf16 v[24:27], v[72:75], v[164:167], v[24:27]\n" \
"v_mfma_f32_16x16x32_bf16 v[28:31], v[76:79], v[164:167], v[28:31]\n" \
"s_waitcnt lgkmcnt(2)\n" \
"v_mfma_f32_16x16x32_bf16 v[32:35], v[72:75], v[168:171], v[32:35]\n" \
"v_mfma_f32_16x16x32_bf16 v[36:39], v[76:79], v[168:171], v[36:39]\n" \
"s_waitcnt lgkmcnt(0)\n" \
"v_mfma_f32_16x16x32_bf16 v[40:43], v[72:75], v[172:175], v[40:43]\n" \
"v_mfma_f32_16x16x32_bf16 v[44:47], v[76:79], v[172:175], v[44:47]\n" \
"s_nop 15\n" \
"s_nop 7\n" \
    : "={v[0:15]}"(acc[0]), "={v[16:31]}"(acc[1]), "={v[32:47]}"(acc[2]), [sX] "+&s"(sX_), [sW] "+&s"(sW_), [cnt] "=&s"(cnt_), [u] "+&s"(u_), [tp] "=&s"(tp_) : "{v182}"(vxd[0]), "{v183}"(vwd[0]), "{v184}"(vwd[1]), "{v185}"(vwd[2]), "{v176}"(vxr[0] + 0u), "{v177}"(vxr[0] + 65536u), "{v178}"(vwr[0] + 0u), "{v179}"(vwr[0] + 65536u), "{v180}"(vwr[1] + 0u), "{v181}"(vwr[1] + 65536u), [rX] "s"(rX_), [rW] "s"(rW_), [m0x] "s"(m0x_), [m0w] "s"(m0w_), [rot] "s"(rot_) : "memory", "scc", "v48", "v49", "v50", "v51", "v52", "v53", "v54", "v55", "v56", "v57", "v58", "v59", "v60", "v61", "v62", "v63", "v80", "v81", "v82", "v83", "v84", "v85", "v86", "v87", "v88", "v89", "v90", "v91", "v92", "v93", "v94", "v95", "v96", "v97", "v98", "v99", "v100", "v101", "v102", "v103", "v104", "v105", "v106", "v107", "v108", "v109", "v110", "v111", "v112", "v113", "v114", "v115", "v116", "v117", "v118", "v119", "v120", "v121", "v122", "v123", "v124", "v125", "v126", "v127", "v64", "v65", "v66", "v67", "v68", "v69", "v70", "v71", "v72", "v73", "v74", "v75", "v76", "v77", "v78", "v79", "v128", "v129", "v130", "v131", "v132", "v133", "v134", "v135", "v136", "v137", "v138", "v139", "v140", "v141", "v142", "v143", "v144", "v145", "v146", "v147", "v148", "v149", "v150", "v151", "v152", "v153", "v154", "v155", "v156", "v157", "v158", "v159", "v160", "v161", "v162", "v163", "v164", "v165", "v166", "v167", "v168", "v169", "v170", "v171", "v172", "v173", "v174", "v175")

#define WO_STAGE 24576
#define WO_LDS 98304
#define WO_PROW(vwd, rW_, m0w_, s0_, s1_) asm volatile( \
"s_add_u32 m0, %[m0w], 16384\n" \
"s_nop 0\n" \
"buffer_load_dwordx4 v87, %[rW], %[s0] offen offset:0 lds\n" \
"s_add_u32 m0, %[m0w], 40960\n" \
"s_nop 0\n" \
"buffer_load_dwordx4 v87, %[rW], %[s1] offen offset:0 lds\n" \
    :: "{v87}"(vwd[0]), [rW] "s"(rW_), [m0w] "s"(m0w_), [s0] "s"(s0_), [s1] "s"(s1_) : "memory", "scc")
#define WO_PROX(vxd, rX_, m0x_, s0_, s1_) asm volatile( \
"s_add_u32 m0, %[m0x], 0\n" \
"s_nop 0\n" \
"buffer_load_dwordx4 v86, %[rX], %[s0] offen offset:0 sc1 lds\n" \
"buffer_load_dwordx4 v86, %[rX], %[s0] offen offset:1024 sc1 lds\n" \
"s_add_u32 m0, %[m0x], 24576\n" \
"s_nop 0\n" \
"buffer_load_dwordx4 v86, %[rX], %[s1] offen offset:0 sc1 lds\n" \
"buffer_load_dwordx4 v86, %[rX], %[s1] offen offset:1024 sc1 lds\n" \
    :: "{v86}"(vxd[0]), [rX] "s"(rX_), [m0x] "s"(m0x_), [s0] "s"(s0_), [s1] "s"(s1_) : "memory", "scc")
#define WO_MAIN(acc, vxd, vwd, vxr, vwr, rX_, rW_, sX_, sW_, m0x_, m0w_, cnt_, u_, rot_, tp_) asm volatile( \
"s_waitcnt lgkmcnt(0)\n" \
"s_waitcnt vmcnt(2)\n" \
"s_barrier\n" \
"ds_read_b64_tr_b16 v[48:49], v82 offset:16384\n" \
"ds_read_b64_tr_b16 v[50:51], v82 offset:16896\n" \
"s_add_u32 m0, %[m0x], 49152\n" \
"s_nop 0\n" \
"buffer_load_dwordx4 v86, %[rX], %[sX] offen offset:0 sc1 lds\n" \
"ds_read_b128 v[16:19], v80 offset:0\n" \
"ds_read_b128 v[20:23], v80 offset:256\n" \
"ds_read_b64_tr_b16 v[52:53], v84 offset:16384\n" \
"buffer_load_dwordx4 v86, %[rX], %[sX] offen offset:1024 sc1 lds\n" \
"ds_read_b64_tr_b16 v[54:55], v84 offset:16896\n" \
"ds_read_b64_tr_b16 v[56:57], v82 offset:20480\n" \
"ds_read_b64_tr_b16 v[58:59], v82 offset:20992\n" \
"s_add_u32 m0, %[m0w], 65536\n" \
"s_nop 0\n" \
"buffer_load_dwordx4 v87, %[rW], %[sW] offen offset:0 lds\n" \
"s_add_u32 %[u], %[u], 1\n" \
"s_add_u32 %[tp], %[u], %[rot]\n" \
"s_and_b32 %[tp], %[tp], 7\n" \
"s_and_b32 %[sX], %[u], 2147483640\n" \
"s_or_b32 %[tp], %[tp], %[sX]\n" \
"s_mul_i32 %[sX], %[tp], 4096\n" \
"s_mul_i32 %[sW], %[tp], 131072\n" \
"ds_read_b128 v[24:27], v80 offset:2048\n" \
"ds_read_b128 v[28:31], v80 offset:2304\n" \
"ds_read_b64_tr_b16 v[60:61], v84 offset:20480\n" \
"s_add_u32 m0, %[m0x], 73728\n" \
"s_nop 0\n" \
"buffer_load_dwordx4 v86, %[rX], %[sX] offen offset:0 sc1 lds\n" \
"ds_read_b64_tr_b16 v[62:63], v84 offset:20992\n" \
"buffer_load_dwordx4 v86, %[rX], %[sX] offen offset:1024 sc1 lds\n" \
"s_add_u32 m0, %[m0w], 90112\n" \
"s_nop 0\n" \
"buffer_load_dwordx4 v87, %[rW], %[sW] offen offset:0 lds\n" \
"s_add_u32 %[u], %[u], 1\n" \
"s_add_u32 %[tp], %[u], %[rot]\n" \
"s_and_b32 %[tp], %[tp], 7\n" \
"s_and_b32 %[sX], %[u], 2147483640\n" \
"s_or_b32 %[tp], %[tp], %[sX]\n" \
"s_mul_i32 %[sX], %[tp], 4096\n" \
"s_mul_i32 %[sW], %[tp], 131072\n" \
"s_waitcnt vmcnt(6) lgkmcnt(0)\n" \
"s_barrier\n" \
"v_mfma_f32_16x16x32_bf16 v[0:3], v[48:51], v[16:19], 0\n" \
"ds_read_b64_tr_b16 v[64:65], v82 offset:40960\n" \
"ds_read_b64_tr_b16 v[66:67], v82 offset:41472\n" \
"ds_read_b128 v[32:35], v80 offset:24576\n" \
"v_mfma_f32_16x16x32_bf16 v[4:7], v[48:51], v[20:23], 0\n" \
"s_add_u32 m0, %[m0x], 0\n" \
"s_nop 0\n" \
"buffer_load_dwordx4 v86, %[rX], %[sX] offen offset:0 sc1 lds\n" \
"ds_read_b128 v[36:39], v80 offset:24832\n" \
"ds_read_b64_tr_b16 v[68:69], v84 offset:40960\n" \
"v_mfma_f32_16x16x32_bf16 v[8:11], v[52:55], v[16:19], 0\n" \
"ds_read_b64_tr_b16 v[70:71], v84 offset:41472\n" \
"ds_read_b64_tr_b16 v[72:73], v82 offset:45056\n" \
"v_mfma_f32_16x16x32_bf16 v[12:15], v[52:55], v[20:23], 0\n" \
"ds_read_b64_tr_b16 v[74:75], v82 offset:45568\n" \
"ds_read_b128 v[40:43], v80 offset:26624\n" \
"v_mfma_f32_16x16x32_bf16 v[0:3], v[56:59], v[24:27], v[0:3]\n" \
"buffer_load_dwordx4 v86, %[rX], %[sX] offen offset:1024 sc1 lds\n" \
"ds_read_b128 v[44:47], v80 offset:26880\n" \
"ds_read_b64_tr_b16 v[76:77], v84 offset:45056\n" \
"v_mfma_f32_16x16x32_bf16 v[4:7], v[56:59], v[28:31], v[4:7]\n" \
"ds_read_b64_tr_b16 v[78:79], v84 offset:45568\n" \
"v_mfma_f32_16x16x32_bf16 v[8:11], v[60:63], v[24:27], v[8:11]\n" \
"s_add_u32 m0, %[m0w], 16384\n" \
"s_nop 0\n" \
"buffer_load_dwordx4 v87, %[rW], %[sW] offen offset:0 lds\n" \
"s_add_u32 %[u], %[u], 1\n" \
"s_add_u32 %[tp], %[u], %[rot]\n" \
"s_and_b32 %[tp], %[tp], 7\n" \
"s_and_b32 %[sX], %[u], 2147483640\n" \
"s_or_b32 %[tp], %[tp], %[sX]\n" \
"s_mul_i32 %[sX], %[tp], 4096\n" \
"s_mul_i32 %[sW], %[tp], 131072\n" \
"v_mfma_f32_16x16x32_bf16 v[12:15], v[60:63], v[28:31], v[12:15]\n" \
"s_waitcnt vmcnt(6) lgkmcnt(0)\n" \
"s_barrier\n" \
"s_mov_b32 %[cnt], 2\n" \
"LOOP_%=:\n" \
"v_mfma_f32_16x16x32_bf16 v[0:3], v[64:67], v[32:35], v[0:3]\n" \
"ds_read_b64_tr_b16 v[48:49], v83 offset:0\n" \
"ds_read_b64_tr_b16 v[50:51], v83 offset:512\n" \
"ds_read_b128 v[16:19], v80 offset:49152\n" \
"v_mfma_f32_16x16x32_bf16 v[4:7], v[64:67], v[36:39], v[4:7]\n" \
"s_add_u32 m0, %[m0x], 24576\n" \
"s_nop 0\n" \
"buffer_load_dwordx4 v86, %[rX], %[sX] offen offset:0 sc1 lds\n" \
"ds_read_b128 v[20:23], v80 offset:49408\n" \
"ds_read_b64_tr_b16 v[52:53], v85 offset:0\n" \
"v_mfma_f32_16x16x32_bf16 v[8:11], v[68:71], v[32:35], v[8:11]\n" \
"ds_read_b64_tr_b16 v[54:55], v85 offset:512\n" \
"ds_read_b64_tr_b16 v[56:57], v83 offset:4096\n" \
"v_mfma_f32_16x16x32_bf16 v[12:15], v[68:71], v[36:39], v[12:15]\n" \
"ds_read_b64_tr_b16 v[58:59], v83 offset:4608\n" \
"ds_read_b128 v[24:27], v80 offset:51200\n" \
"v_mfma_f32_16x16x32_bf16 v[0:3], v[72:75], v[40:43], v[0:3]\n" \
"buffer_load_dwordx4 v86, %[rX], %[sX] offen offset:1024 sc1 lds\n" \
"ds_read_b128 v[28:31], v80 offset:51456\n" \
"ds_read_b64_tr_b16 v[60:61], v85 offset:4096\n" \
"v_mfma_f32_16x16x32_bf16 v[4:7], v[72:75], v[44:47], v[4:7]\n" \
"ds_read_b64_tr_b16 v[62:63], v85 offset:4608\n" \
"v_mfma_f32_16x16x32_bf16 v[8:11], v[76:79], v[40:43], v[8:11]\n" \
"s_add_u32 m0, %[m0w], 40960\n" \
"s_nop 0\n" \
"buffer_load_dwordx4 v87, %[rW], %[sW] offen offset:0 lds\n" \
"s_add_u32 %[u], %[u], 1\n" \
"s_add_u32 %[tp], %[u], %[rot]\n" \
"s_and_b32 %[tp], %[tp], 7\n" \
"s_and_b32 %[sX], %[u], 2147483640\n" \
"s_or_b32 %[tp], %[tp], %[sX]\n" \
"s_mul_i32 %[sX], %[tp], 4096\n" \
"s_mul_i32 %[sW], %[tp], 131072\n" \
"v_mfma_f32_16x16x32_bf16 v[12:15], v[76:79], v[44:47], v[12:15]\n" \
"s_waitcnt vmcnt(6) lgkmcnt(0)\n" \
"s_barrier\n" \
"v_mfma_f32_16x16x32_bf16 v[0:3], v[48:51], v[16:19], v[0:3]\n" \
"ds_read_b64_tr_b16 v[64:65], v83 offset:24576\n" \
"ds_read_b64_tr_b16 v[66:67], v83 offset:25088\n" \
"ds_read_b128 v[32:35], v81 offset:8192\n" \
"v_mfma_f32_16x16x32_bf16 v[4:7], v[48:51], v[20:23], v[4:7]\n" \
"s_add_u32 m0, %[m0x], 49152\n" \
"s_nop 0\n" \
"buffer_load_dwordx4 v86, %[rX], %[sX] offen offset:0 sc1 lds\n" \
"ds_read_b128 v[36:39], v81 offset:8448\n" \
"ds_read_b64_tr_b16 v[68:69], v85 offset:24576\n" \
"v_mfma_f32_16x16x32_bf16 v[8:11], v[52:55], v[16:19], v[8:11]\n" \
"ds_read_b64_tr_b16 v[70:71], v85 offset:25088\n" \
"ds_read_b64_tr_b16 v[72:73], v83 offset:28672\n" \
"v_mfma_f32_16x16x32_bf16 v[12:15], v[52:55], v[20:23], v[12:15]\n" \
"ds_read_b64_tr_b16 v[74:75], v83 offset:29184\n" \
"ds_read_b128 v[40:43], v81 offset:10240\n" \
"v_mfma_f32_16x16x32_bf16 v[0:3], v[56:59], v[24:27], v[0:3]\n" \
"buffer_load_dwordx4 v86, %[rX], %[sX] offen offset:1024 sc1 lds\n" \
"ds_read_b128 v[44:47], v81 offset:10496\n" \
"ds_read_b64_tr_b16 v[76:77], v85 offset:28672\n" \
"v_mfma_f32_16x16x32_bf16 v[4:7], v[56:59], v[28:31], v[4:7]\n" \
"ds_read_b64_tr_b16 v[78:79], v85 offset:29184\n" \
"v_mfma_f32_16x16x32_bf16 v[8:11], v[60:63], v[24:27], v[8:11]\n" \
"s_add_u32 m0, %[m0w], 65536\n" \
"s_nop 0\n" \
"buffer_load_dwordx4 v87, %[rW], %[sW] offen offset:0 lds\n" \
"s_add_u32 %[u], %[u], 1\n" \
"s_add_u32 %[tp], %[u], %[rot]\n" \
"s_and_b32 %[tp], %[tp], 7\n" \
"s_and_b32 %[sX], %[u], 2147483640\n" \
"s_or_b32 %[tp], %[tp], %[sX]\n" \
"s_mul_i32 %[sX], %[tp], 4096\n" \
"s_mul_i32 %[sW], %[tp], 131072\n" \
"v_mfma_f32_16x16x32_bf16 v[12:15], v[60:63], v[28:31], v[12:15]\n" \
"s_waitcnt vmcnt(6) lgkmcnt(0)\n" \
"s_barrier\n" \
"v_mfma_f32_16x16x32_bf16 v[0:3], v[64:67], v[32:35], v[0:3]\n" \
"ds_read_b64_tr_b16 v[48:49], v82 offset:16384\n" \
"ds_read_b64_tr_b16 v[50:51], v82 offset:16896\n" \
"ds_read_b128 v[16:19], v80 offset:0\n" \
"v_mfma_f32_16x16x32_bf16 v[4:7], v[64:67], v[36:39], v[4:7]\n" \
"s_add_u32 m0, %[m0x], 73728\n" \
"s_nop 0\n" \
"buffer_load_dwordx4 v86, %[rX], %[sX] offen offset:0 sc1 lds\n" \
"ds_read_b128 v[20:23], v80 offset:256\n" \
"ds_read_b64_tr_b16 v[52:53], v84 offset:16384\n" \
"v_mfma_f32_16x16x32_bf16 v[8:11], v[68:71], v[32:35], v[8:11]\n" \
"ds_read_b64_tr_b16 v[54:55], v84 offset:16896\n" \
"ds_read_b64_tr_b16 v[56:57], v82 offset:20480\n" \
"v_mfma_f32_16x16x32_bf16 v[12:15], v[68:71], v[36:39], v[12:15]\n" \
"ds_read_b64_tr_b16 v[58:59], v82 offset:20992\n" \
"ds_read_b128 v[24:27], v80 offset:2048\n" \
"v_mfma_f32_16x16x32_bf16 v[0:3], v[72:75], v[40:43], v[0:3]\n" \
"buffer_load_dwordx4 v86, %[rX], %[sX] offen offset:1024 sc1 lds\n" \
"ds_read_b128 v[28:31], v80 offset:2304\n" \
"ds_read_b64_tr_b16 v[60:61], v84 offset:20480\n" \
"v_mfma_f32_16x16x32_bf16 v[4:7], v[72:75], v[44:47], v[4:7]\n" \
"ds_read_b64_tr_b16 v[62:63], v84 offset:20992\n" \
"v_mfma_f32_16x16x32_bf16 v[8:11], v[76:79], v[40:43], v[8:11]\n" \
"s_add_u32 m0, %[m0w], 90112\n" \
"s_nop 0\n" \
"buffer_load_dwordx4 v87, %[rW], %[sW] offen offset:0 lds\n" \
"s_add_u32 %[u], %[u], 1\n" \
"s_add_u32 %[tp], %[u], %[rot]\n" \
"s_and_b32 %[tp], %[tp], 7\n" \
"s_and_b32 %[sX], %[u], 2147483640\n" \
"s_or_b32 %[tp], %[tp], %[sX]\n" \
"s_mul_i32 %[sX], %[tp], 4096\n" \
"s_mul_i32 %[sW], %[tp], 131072\n" \
"v_mfma_f32_16x16x32_bf16 v[12:15], v[76:79], v[44:47], v[12:15]\n" \
"s_waitcnt vmcnt(6) lgkmcnt(0)\n" \
"s_barrier\n" \
"v_mfma_f32_16x16x32_bf16 v[0:3], v[48:51], v[16:19], v[0:3]\n" \
"ds_read_b64_tr_b16 v[64:65], v82 offset:40960\n" \
"ds_read_b64_tr_b16 v[66:67], v82 offset:41472\n" \
"ds_read_b128 v[32:35], v80 offset:24576\n" \
"v_mfma_f32_16x16x32_bf16 v[4:7], v[48:51], v[20:23], v[4:7]\n" \
"s_add_u32 m0, %[m0x], 0\n" \
"s_nop 0\n" \
"buffer_load_dwordx4 v86, %[rX], %[sX] offen offset:0 sc1 lds\n" \
"ds_read_b128 v[36:39], v80 offset:24832\n" \
"ds_read_b64_tr_b16 v[68:69], v84 offset:40960\n" \
"v_mfma_f32_16x16x32_bf16 v[8:11], v[52:55], v[16:19], v[8:11]\n" \
"ds_read_b64_tr_b16 v[70:71], v84 offset:41472\n" \
"ds_read_b64_tr_b16 v[72:73], v82 offset:45056\n" \
"v_mfma_f32_16x16x32_bf16 v[12:15], v[52:55], v[20:23], v[12:15]\n" \
"ds_read_b64_tr_b16 v[74:75], v82 offset:45568\n" \
"ds_read_b128 v[40:43], v80 offset:26624\n" \
"v_mfma_f32_16x16x32_bf16 v[0:3], v[56:59], v[24:27], v[0:3]\n" \
"buffer_load_dwordx4 v86, %[rX], %[sX] offen offset:1024 sc1 lds\n" \
"ds_read_b128 v[44:47], v80 offset:26880\n" \
"ds_read_b64_tr_b16 v[76:77], v84 offset:45056\n" \
"v_mfma_f32_16x16x32_bf16 v[4:7], v[56:59], v[28:31], v[4:7]\n" \
"ds_read_b64_tr_b16 v[78:79], v84 offset:45568\n" \
"v_mfma_f32_16x16x32_bf16 v[8:11], v[60:63], v[24:27], v[8:11]\n" \
"s_add_u32 m0, %[m0w], 16384\n" \
"s_nop 0\n" \
"buffer_load_dwordx4 v87, %[rW], %[sW] offen offset:0 lds\n" \
"s_add_u32 %[u], %[u], 1\n" \
"s_add_u32 %[tp], %[u], %[rot]\n" \
"s_and_b32 %[tp], %[tp], 7\n" \
"s_and_b32 %[sX], %[u], 2147483640\n" \
"s_or_b32 %[tp], %[tp], %[sX]\n" \
"s_mul_i32 %[sX], %[tp], 4096\n" \
"s_mul_i32 %[sW], %[tp], 131072\n" \
"v_mfma_f32_16x16x32_bf16 v[12:15], v[60:63], v[28:31], v[12:15]\n" \
"s_waitcnt vmcnt(6) lgkmcnt(0)\n" \
"s_barrier\n" \
"s_sub_u32 %[cnt], %[cnt], 1\n" \
"s_cmp_lg_u32 %[cnt], 0\n" \
"s_cbranch_scc1 LOOP_%=\n" \
"v_mfma_f32_16x16x32_bf16 v[0:3], v[64:67], v[32:35], v[0:3]\n" \
"ds_read_b64_tr_b16 v[48:49], v83 offset:0\n" \
"ds_read_b64_tr_b16 v[50:51], v83 offset:512\n" \
"ds_read_b128 v[16:19], v80 offset:49152\n" \
"v_mfma_f32_16x16x32_bf16 v[4:7], v[64:67], v[36:39], v[4:7]\n" \
"s_add_u32 m0, %[m0x], 24576\n" \
"s_nop 0\n" \
"buffer_load_dwordx4 v86, %[rX], %[sX] offen offset:0 sc1 lds\n" \
"ds_read_b128 v[20:23], v80 offset:49408\n" \
"ds_read_b64_tr_b16 v[52:53], v85 offset:0\n" \
"v_mfma_f32_16x16x32_bf16 v[8:11], v[68:71], v[32:35], v[8:11]\n" \
"ds_read_b64_tr_b16 v[54:55], v85 offset:512\n" \
"ds_read_b64_tr_b16 v[56:57], v83 offset:4096\n" \
"v_mfma_f32_16x16x32_bf16 v[12:15], v[68:71], v[36:39], v[12:15]\n" \
"ds_read_b64_tr_b16 v[58:59], v83 offset:4608\n" \
"ds_read_b128 v[24:27], v80 offset:51200\n" \
"v_mfma_f32_16x16x32_bf16 v[0:3], v[72:75], v[40:43], v[0:3]\n" \
"buffer_load_dwordx4 v86, %[rX], %[sX] offen offset:1024 sc1 lds\n" \
"ds_read_b128 v[28:31], v80 offset:51456\n" \
"ds_read_b64_tr_b16 v[60:61], v85 offset:4096\n" \
"v_mfma_f32_16x16x32_bf16 v[4:7], v[72:75], v[44:47], v[4:7]\n" \
"ds_read_b64_tr_b16 v[62:63], v85 offset:4608\n" \
"v_mfma_f32_16x16x32_bf16 v[8:11], v[76:79], v[40:43], v[8:11]\n" \
"s_add_u32 m0, %[m0w], 40960\n" \
"s_nop 0\n" \
"buffer_load_dwordx4 v87, %[rW], %[sW] offen offset:0 lds\n" \
"s_add_u32 %[u], %[u], 1\n" \
"s_add_u32 %[tp], %[u], %[rot]\n" \
"s_and_b32 %[tp], %[tp], 7\n" \
"s_and_b32 %[sX], %[u], 2147483640\n" \
"s_or_b32 %[tp], %[tp], %[sX]\n" \
"s_mul_i32 %[sX], %[tp], 4096\n" \
"s_mul_i32 %[sW], %[tp], 131072\n" \
"v_mfma_f32_16x16x32_bf16 v[12:15], v[76:79], v[44:47], v[12:15]\n" \
"s_waitcnt vmcnt(6) lgkmcnt(0)\n" \
"s_barrier\n" \
"v_mfma_f32_16x16x32_bf16 v[0:3], v[48:51], v[16:19], v[0:3]\n" \
"ds_read_b64_tr_b16 v[64:65], v83 offset:24576\n" \
"ds_read_b64_tr_b16 v[66:67], v83 offset:25088\n" \
"ds_read_b128 v[32:35], v81 offset:8192\n" \
"v_mfma_f32_16x16x32_bf16 v[4:7], v[48:51], v[20:23], v[4:7]\n" \
"s_add_u32 m0, %[m0x], 49152\n" \
"s_nop 0\n" \
"buffer_load_dwordx4 v86, %[rX], %[sX] offen offset:0 sc1 lds\n" \
"ds_read_b128 v[36:39], v81 offset:8448\n" \
"ds_read_b64_tr_b16 v[68:69], v85 offset:24576\n" \
"v_mfma_f32_16x16x32_bf16 v[8:11], v[52:55], v[16:19], v[8:11]\n" \
"ds_read_b64_tr_b16 v[70:71], v85 offset:25088\n" \
"ds_read_b64_tr_b16 v[72:73], v83 offset:28672\n" \
"v_mfma_f32_16x16x32_bf16 v[12:15], v[52:55], v[20:23], v[12:15]\n" \
"ds_read_b64_tr_b16 v[74:75], v83 offset:29184\n" \
"ds_read_b128 v[40:43], v81 offset:10240\n" \
"v_mfma_f32_16x16x32_bf16 v[0:3], v[56:59], v[24:27], v[0:3]\n" \
"buffer_load_dwordx4 v86, %[rX], %[sX] offen offset:1024 sc1 lds\n" \
"ds_read_b128 v[44:47], v81 offset:10496\n" \
"ds_read_b64_tr_b16 v[76:77], v85 offset:28672\n" \
"v_mfma_f32_16x16x32_bf16 v[4:7], v[56:59], v[28:31], v[4:7]\n" \
"ds_read_b64_tr_b16 v[78:79], v85 offset:29184\n" \
"v_mfma_f32_16x16x32_bf16 v[8:11], v[60:63], v[24:27], v[8:11]\n" \
"s_add_u32 m0, %[m0w], 65536\n" \
"s_nop 0\n" \
"buffer_load_dwordx4 v87, %[rW], %[sW] offen offset:0 lds\n" \
"s_add_u32 %[u], %[u], 1\n" \
"s_add_u32 %[tp], %[u], %[rot]\n" \
"s_and_b32 %[tp], %[tp], 7\n" \
"s_and_b32 %[sX], %[u], 2147483640\n" \
"s_or_b32 %[tp], %[tp], %[sX]\n" \
"s_mul_i32 %[sX], %[tp], 4096\n" \
"s_mul_i32 %[sW], %[tp], 131072\n" \
"v_mfma_f32_16x16x32_bf16 v[12:15], v[60:63], v[28:31], v[12:15]\n" \
"s_waitcnt vmcnt(6) lgkmcnt(0)\n" \
"s_barrier\n" \
"v_mfma_f32_16x16x32_bf16 v[0:3], v[64:67], v[32:35], v[0:3]\n" \
"ds_read_b64_tr_b16 v[48:49], v82 offset:16384\n" \
"ds_read_b64_tr_b16 v[50:51], v82 offset:16896\n" \
"ds_read_b128 v[16:19], v80 offset:0\n" \
"v_mfma_f32_16x16x32_bf16 v[4:7], v[64:67], v[36:39], v[4:7]\n" \
"s_add_u32 m0, %[m0x], 73728\n" \
"s_nop 0\n" \
"buffer_load_dwordx4 v86, %[rX], %[sX] offen offset:0 sc1 lds\n" \
"ds_read_b128 v[20:23], v80 offset:256\n" \
"ds_read_b64_tr_b16 v[52:53], v84 offset:16384\n" \
"v_mfma_f32_16x16x32_bf16 v[8:11], v[68:71], v[32:35], v[8:11]\n" \
"ds_read_b64_tr_b16 v[54:55], v84 offset:16896\n" \
"ds_read_b64_tr_b16 v[56:57], v82 offset:20480\n" \
"v_mfma_f32_16x16x32_bf16 v[12:15], v[68:71], v[36:39], v[12:15]\n" \
"ds_read_b64_tr_b16 v[58:59], v82 offset:20992\n" \
"ds_read_b128 v[24:27], v80 offset:2048\n" \
"v_mfma_f32_16x16x32_bf16 v[0:3], v[72:75], v[40:43], v[0:3]\n" \
"buffer_load_dwordx4 v86, %[rX], %[sX] offen offset:1024 sc1 lds\n" \
"ds_read_b128 v[28:31], v80 offset:2304\n" \
"ds_read_b64_tr_b16 v[60:61], v84 offset:20480\n" \
"v_mfma_f32_16x16x32_bf16 v[4:7], v[72:75], v[44:47], v[4:7]\n" \
"ds_read_b64_tr_b16 v[62:63], v84 offset:20992\n" \
"v_mfma_f32_16x16x32_bf16 v[8:11], v[76:79], v[40:43], v[8:11]\n" \
"s_add_u32 m0, %[m0w], 90112\n" \
"s_nop 0\n" \
"buffer_load_dwordx4 v87, %[rW], %[sW] offen offset:0 lds\n" \
"s_add_u32 %[u], %[u], 1\n" \
"s_add_u32 %[tp], %[u], %[rot]\n" \
"s_and_b32 %[tp], %[tp], 7\n" \
"s_and_b32 %[sX], %[u], 2147483640\n" \
"s_or_b32 %[tp], %[tp], %[sX]\n" \
"s_mul_i32 %[sX], %[tp], 4096\n" \
"s_mul_i32 %[sW], %[tp], 131072\n" \
"v_mfma_f32_16x16x32_bf16 v[12:15], v[76:79], v[44:47], v[12:15]\n" \
"s_waitcnt vmcnt(6) lgkmcnt(0)\n" \
"s_barrier\n" \
"v_mfma_f32_16x16x32_bf16 v[0:3], v[48:51], v[16:19], v[0:3]\n" \
"ds_read_b64_tr_b16 v[64:65], v82 offset:40960\n" \
"ds_read_b64_tr_b16 v[66:67], v82 offset:41472\n" \
"ds_read_b128 v[32:35], v80 offset:24576\n" \
"v_mfma_f32_16x16x32_bf16 v[4:7], v[48:51], v[20:23], v[4:7]\n" \
"ds_read_b128 v[36:39], v80 offset:24832\n" \
"ds_read_b64_tr_b16 v[68:69], v84 offset:40960\n" \
"v_mfma_f32_16x16x32_bf16 v[8:11], v[52:55], v[16:19], v[8:11]\n" \
"ds_read_b64_tr_b16 v[70:71], v84 offset:41472\n" \
"ds_read_b64_tr_b16 v[72:73], v82 offset:45056\n" \
"v_mfma_f32_16x16x32_bf16 v[12:15], v[52:55], v[20:23], v[12:15]\n" \
"ds_read_b64_tr_b16 v[74:75], v82 offset:45568\n" \
"ds_read_b128 v[40:43], v80 offset:26624\n" \
"v_mfma_f32_16x16x32_bf16 v[0:3], v[56:59], v[24:27], v[0:3]\n" \
"ds_read_b128 v[44:47], v80 offset:26880\n" \
"ds_read_b64_tr_b16 v[76:77], v84 offset:45056\n" \
"v_mfma_f32_16x16x32_bf16 v[4:7], v[56:59], v[28:31], v[4:7]\n" \
"ds_read_b64_tr_b16 v[78:79], v84 offset:45568\n" \
"v_mfma_f32_16x16x32_bf16 v[8:11], v[60:63], v[24:27], v[8:11]\n" \
"v_mfma_f32_16x16x32_bf16 v[12:15], v[60:63], v[28:31], v[12:15]\n" \
"s_waitcnt vmcnt(3) lgkmcnt(0)\n" \
"s_barrier\n" \
"v_mfma_f32_16x16x32_bf16 v[0:3], v[64:67], v[32:35], v[0:3]\n" \
"ds_read_b64_tr_b16 v[48:49], v83 offset:0\n" \
"ds_read_b64_tr_b16 v[50:51], v83 offset:512\n" \
"ds_read_b128 v[16:19], v80 offset:49152\n" \
"v_mfma_f32_16x16x32_bf16 v[4:7], v[64:67], v[36:39], v[4:7]\n" \
"ds_read_b128 v[20:23], v80 offset:49408\n" \
"ds_read_b64_tr_b16 v[52:53], v85 offset:0\n" \
"v_mfma_f32_16x16x32_bf16 v[8:11], v[68:71], v[32:35], v[8:11]\n" \
"ds_read_b64_tr_b16 v[54:55], v85 offset:512\n" \
"ds_read_b64_tr_b16 v[56:57], v83 offset:4096\n" \
"v_mfma_f32_16x16x32_bf16 v[12:15], v[68:71], v[36:39], v[12:15]\n" \
"ds_read_b64_tr_b16 v[58:59], v83 offset:4608\n" \
"ds_read_b128 v[24:27], v80 offset:51200\n" \
"v_mfma_f32_16x16x32_bf16 v[0:3], v[72:75], v[40:43], v[0:3]\n" \
"ds_read_b128 v[28:31], v80 offset:51456\n" \
"ds_read_b64_tr_b16 v[60:61], v85 offset:4096\n" \
"v_mfma_f32_16x16x32_bf16 v[4:7], v[72:75], v[44:47], v[4:7]\n" \
"ds_read_b64_tr_b16 v[62:63], v85 offset:4608\n" \
"v_mfma_f32_16x16x32_bf16 v[8:11], v[76:79], v[40:43], v[8:11]\n" \
"v_mfma_f32_16x16x32_bf16 v[12:15], v[76:79], v[44:47], v[12:15]\n" \
"s_waitcnt vmcnt(0) lgkmcnt(0)\n" \
"s_barrier\n" \
"v_mfma_f32_16x16x32_bf16 v[0:3], v[48:51], v[16:19], v[0:3]\n" \
"ds_read_b64_tr_b16 v[64:65], v83 offset:24576\n" \
"ds_read_b64_tr_b16 v[66:67], v83 offset:25088\n" \
"ds_read_b128 v[32:35], v81 offset:8192\n" \
"v_mfma_f32_16x16x32_bf16 v[4:7], v[48:51], v[20:23], v[4:7]\n" \
"ds_read_b128 v[36:39], v81 offset:8448\n" \
"ds_read_b64_tr_b16 v[68:69], v85 offset:24576\n" \
"v_mfma_f32_16x16x32_bf16 v[8:11], v[52:55], v[16:19], v[8:11]\n" \
"ds_read_b64_tr_b16 v[70:71], v85 offset:25088\n" \
"ds_read_b64_tr_b16 v[72:73], v83 offset:28672\n" \
"v_mfma_f32_16x16x32_bf16 v[12:15], v[52:55], v[20:23], v[12:15]\n" \
"ds_read_b64_tr_b16 v[74:75], v83 offset:29184\n" \
"ds_read_b128 v[40:43], v81 offset:10240\n" \
"v_mfma_f32_16x16x32_bf16 v[0:3], v[56:59], v[24:27], v[0:3]\n" \
"ds_read_b128 v[44:47], v81 offset:10496\n" \
"ds_read_b64_tr_b16 v[76:77], v85 offset:28672\n" \
"v_mfma_f32_16x16x32_bf16 v[4:7], v[56:59], v[28:31], v[4:7]\n" \
"ds_read_b64_tr_b16 v[78:79], v85 offset:29184\n" \
"v_mfma_f32_16x16x32_bf16 v[8:11], v[60:63], v[24:27], v[8:11]\n" \
"v_mfma_f32_16x16x32_bf16 v[12:15], v[60:63], v[28:31], v[12:15]\n" \
"s_waitcnt lgkmcnt(9)\n" \
"v_mfma_f32_16x16x32_bf16 v[0:3], v[64:67], v[32:35], v[0:3]\n" \
"s_waitcnt lgkmcnt(8)\n" \
"v_mfma_f32_16x16x32_bf16 v[4:7], v[64:67], v[36:39], v[4:7]\n" \
"s_waitcnt lgkmcnt(6)\n" \
"v_mfma_f32_16x16x32_bf16 v[8:11], v[68:71], v[32:35], v[8:11]\n" \
"v_mfma_f32_16x16x32_bf16 v[12:15], v[68:71], v[36:39], v[12:15]\n" \
"s_waitcnt lgkmcnt(3)\n" \
"v_mfma_f32_16x16x32_bf16 v[0:3], v[72:75], v[40:43], v[0:3]\n" \
"s_waitcnt lgkmcnt(2)\n" \
"v_mfma_f32_16x16x32_bf16 v[4:7], v[72:75], v[44:47], v[4:7]\n" \
"s_waitcnt lgkmcnt(0)\n" \
"v_mfma_f32_16x16x32_bf16 v[8:11], v[76:79], v[40:43], v[8:11]\n" \
"v_mfma_f32_16x16x32_bf16 v[12:15], v[76:79], v[44:47], v[12:15]\n" \
"s_nop 15\n" \
"s_nop 7\n" \
    : "={v[0:15]}"(acc[0]), [sX] "+&s"(sX_), [sW] "+&s"(sW_), [cnt] "=&s"(cnt_), [u] "+&s"(u_), [tp] "=&s"(tp_) : "{v86}"(vxd[0]), "{v87}"(vwd[0]), "{v80}"(vxr[0] + 0u), "{v81}"(vxr[0] + 65536u), "{v82}"(vwr[0] + 0u), "{v83}"(vwr[0] + 65536u), "{v84}"(vwr[1] + 0u), "{v85}"(vwr[1] + 65536u), [rX] "s"(rX_), [rW] "s"(rW_), [m0x] "s"(m0x_), [m0w] "s"(m0w_), [rot] "s"(rot_) : "memory", "scc", "v16", "v17", "v18", "v19", "v20", "v21", "v22", "v23", "v24", "v25", "v26", "v27", "v28", "v29", "v30", "v31", "v48", "v49", "v50", "v51", "v52", "v53", "v54", "v55", "v56", "v57", "v58", "v59", "v60", "v61", "v62", "v63", "v32", "v33", "v34", "v35", "v36", "v37", "v38", "v39", "v40", "v41", "v42", "v43", "v44", "v45", "v46", "v47", "v64", "v65", "v66", "v67", "v68", "v69", "v70", "v71", "v72", "v73", "v74", "v75", "v76", "v77", "v78", "v79")

#define DN_STAGE 32768
#define DN_LDS 131072
#define DN_PROW(vwd, rW_, m0w_, s0_, s1_) asm volatile( \
"s_add_u32 m0, %[m0w], 16384\n" \
"s_nop 0\n" \
"buffer_load_dwordx4 v139, %[rW], %[s0] offen offset:0 lds\n" \
"buffer_load_dwordx4 v140, %[rW], %[s0] offen offset:1024 lds\n" \
"s_add_u32 m0, %[m0w], 49152\n" \
"s_nop 0\n" \
"buffer_load_dwordx4 v139, %[rW], %[s1] offen offset:0 lds\n" \
"buffer_load_dwordx4 v140, %[rW], %[s1] offen offset:1024 lds\n" \
    :: "{v139}"(vwd[0]), "{v140}"(vwd[1]), [rW] "s"(rW_), [m0w] "s"(m0w_), [s0] "s"(s0_), [s1] "s"(s1_) : "memory", "scc")
#define DN_PROX(vxd, rX_, m0x_, s0_, s1_) asm volatile( \
"s_add_u32 m0, %[m0x], 0\n" \
"s_nop 0\n" \
"buffer_load_dwordx4 v138, %[rX], %[s0] offen offset:0 sc1 lds\n" \
"buffer_load_dwordx4 v138, %[rX], %[s0] offen offset:1024 sc1 lds\n" \
"s_add_u32 m0, %[m0x], 32768\n" \
"s_nop 0\n" \
"buffer_load_dwordx4 v138, %[rX], %[s1] offen offset:0 sc1 lds\n" \
"buffer_load_dwordx4 v138, %[rX], %[s1] offen offset:1024 sc1 lds\n" \
    :: "{v138}"(vxd[0]), [rX] "s"(rX_), [m0x] "s"(m0x_), [s0] "s"(s0_), [s1] "s"(s1_) : "memory", "scc")
#define DN_MAIN(acc, vxd, vwd, vxr, vwr, rX_, rW_, sX_, sW_, m0x_, m0w_, cnt_, u_, rot_, tp_) asm volatile( \
"s_waitcnt lgkmcnt(0)\n" \
"s_waitcnt vmcnt(2)\n" \
"s_barrier\n" \
"ds_read_b64_tr_b16 v[64:65], v130 offset:16384\n" \
"ds_read_b64_tr_b16 v[66:67], v130 offset:16896\n" \
"s_add_u32 m0, %[m0x], 65536\n" \
"s_nop 0\n" \
"buffer_load_dwordx4 v138, %[rX], %[sX] offen offset:0 sc1 lds\n" \
"ds_read_b128 v[32:35], v128 offset:0\n" \
"ds_read_b128 v[36:39], v128 offset:256\n" \
"ds_read_b64_tr_b16 v[68:69], v132 offset:16384\n" \
"ds_read_b64_tr_b16 v[70:71], v132 offset:16896\n" \
"buffer_load_dwordx4 v138, %[rX], %[sX] offen offset:1024 sc1 lds\n" \
"ds_read_b64_tr_b16 v[72:73], v134 offset:16384\n" \
"ds_read_b64_tr_b16 v[74:75], v134 offset:16896\n" \
"ds_read_b64_tr_b16 v[76:77], v136 offset:16384\n" \
"s_add_u32 m0, %[m0w], 81920\n" \
"s_nop 0\n" \
"buffer_load_dwordx4 v139, %[rW], %[sW] offen offset:0 lds\n" \
"ds_read_b64_tr_b16 v[78:79], v136 offset:16896\n" \
"ds_read_b64_tr_b16 v[80:81], v130 offset:20480\n" \
"ds_read_b64_tr_b16 v[82:83], v130 offset:20992\n" \
"buffer_load_dwordx4 v140, %[rW], %[sW] offen offset:1024 lds\n" \
"s_add_u32 %[u], %[u], 1\n" \
"s_add_u32 %[tp], %[u], %[rot]\n" \
"s_and_b32 %[tp], %[tp], 3\n" \
"s_and_b32 %[sX], %[u], 2147483644\n" \
"s_or_b32 %[tp], %[tp], %[sX]\n" \
"s_mul_i32 %[sX], %[tp], 4096\n" \
"s_mul_i32 %[sW], %[tp], 131072\n" \
"ds_read_b128 v[40:43], v128 offset:2048\n" \
"ds_read_b128 v[44:47], v128 offset:2304\n" \
"ds_read_b64_tr_b16 v[84:85], v132 offset:20480\n" \
"ds_read_b64_tr_b16 v[86:87], v132 offset:20992\n" \
"s_add_u32 m0, %[m0x], 98304\n" \
"s_nop 0\n" \
"buffer_load_dwordx4 v138, %[rX], %[sX] offen offset:0 sc1 lds\n" \
"ds_read_b64_tr_b16 v[88:89], v134 offset:20480\n" \
"ds_read_b64_tr_b16 v[90:91], v134 offset:20992\n" \
"ds_read_b64_tr_b16 v[92:93], v136 offset:20480\n" \
"buffer_load_dwordx4 v138, %[rX], %[sX] offen offset:1024 sc1 lds\n" \
"ds_read_b64_tr_b16 v[94:95], v136 offset:20992\n" \
"s_add_u32 m0, %[m0w], 114688\n" \
"s_nop 0\n" \
"buffer_load_dwordx4 v139, %[rW], %[sW] offen offset:0 lds\n" \
"buffer_load_dwordx4 v140, %[rW], %[sW] offen offset:1024 lds\n" \
"s_add_u32 %[u], %[u], 1\n" \
"s_add_u32 %[tp], %[u], %[rot]\n" \
"s_and_b32 %[tp], %[tp], 3\n" \
"s_and_b32 %[sX], %[u], 2147483644\n" \
"s_or_b32 %[tp], %[tp], %[sX]\n" \
"s_mul_i32 %[sX], %[tp], 4096\n" \
"s_mul_i32 %[sW], %[tp], 131072\n" \
"s_waitcnt vmcnt(8) lgkmcnt(0)\n" \
"s_barrier\n" \
"v_mfma_f32_16x16x32_bf16 v[0:3], v[64:67], v[32:35], 0\n" \
"ds_read_b64_tr_b16 v[96:97], v130 offset:49152\n" \
"ds_read_b64_tr_b16 v[98:99], v130 offset:49664\n" \
"v_mfma_f32_16x16x32_bf16 v[4:7], v[64:67], v[36:39], 0\n" \
"ds_read_b128 v[48:51], v128 offset:32768\n" \
"ds_read_b128 v[52:55], v128 offset:33024\n" \
"v_mfma_f32_16x16x32_bf16 v[8:11], v[68:71], v[32:35], 0\n" \
"s_add_u32 m0, %[m0x], 0\n" \
"s_nop 0\n" \
"buffer_load_dwordx4 v138, %[rX], %[sX] offen offset:0 sc1 lds\n" \
"ds_read_b64_tr_b16 v[100:101], v132 offset:49152\n" \
"v_mfma_f32_16x16x32_bf16 v[12:15], v[68:71], v[36:39], 0\n" \
"ds_read_b64_tr_b16 v[102:103], v132 offset:49664\n" \
"ds_read_b64_tr_b16 v[104:105], v134 offset:49152\n" \
"v_mfma_f32_16x16x32_bf16 v[16:19], v[72:75], v[32:35], 0\n" \
"ds_read_b64_tr_b16 v[106:107], v134 offset:49664\n" \
"ds_read_b64_tr_b16 v[108:109], v136 offset:49152\n" \
"v_mfma_f32_16x16x32_bf16 v[20:23], v[72:75], v[36:39], 0\n" \
"ds_read_b64_tr_b16 v[110:111], v136 offset:49664\n" \
"v_mfma_f32_16x16x32_bf16 v[24:27], v[76:79], v[32:35], 0\n" \
"ds_read_b64_tr_b16 v[112:113], v130 offset:53248\n" \
"buffer_load_dwordx4 v138, %[rX], %[sX] offen offset:1024 sc1 lds\n" \
"ds_read_b64_tr_b16 v[114:115], v130 offset:53760\n" \
"v_mfma_f32_16x16x32_bf16 v[28:31], v[76:79], v[36:39], 0\n" \
"ds_read_b128 v[56:59], v128 offset:34816\n" \
"ds_read_b128 v[60:63], v128 offset:35072\n" \
"v_mfma_f32_16x16x32_bf16 v[0:3], v[80:83], v[40:43], v[0:3]\n" \
"ds_read_b64_tr_b16 v[116:117], v132 offset:53248\n" \
"v_mfma_f32_16x16x32_bf16 v[4:7], v[80:83], v[44:47], v[4:7]\n" \
"ds_read_b64_tr_b16 v[118:119], v132 offset:53760\n" \
"ds_read_b64_tr_b16 v[120:121], v134 offset:53248\n" \
"v_mfma_f32_16x16x32_bf16 v[8:11], v[84:87], v[40:43], v[8:11]\n" \
"s_add_u32 m0, %[m0w], 16384\n" \
"s_nop 0\n" \
"buffer_load_dwordx4 v139, %[rW], %[sW] offen offset:0 lds\n" \
"ds_read_b64_tr_b16 v[122:123], v134 offset:53760\n" \
"ds_read_b64_tr_b16 v[124:125], v136 offset:53248\n" \
"v_mfma_f32_16x16x32_bf16 v[12:15], v[84:87], v[44:47], v[12:15]\n" \
"ds_read_b64_tr_b16 v[126:127], v136 offset:53760\n" \
"v_mfma_f32_16x16x32_bf16 v[16:19], v[88:91], v[40:43], v[16:19]\n" \
"v_mfma_f32_16x16x32_bf16 v[20:23], v[88:91], v[44:47], v[20:23]\n" \
"v_mfma_f32_16x16x32_bf16 v[24:27], v[92:95], v[40:43], v[24:27]\n" \
"buffer_load_dwordx4 v140, %[rW], %[sW] offen offset:1024 lds\n" \
"s_add_u32 %[u], %[u], 1\n" \
"s_add_u32 %[tp], %[u], %[rot]\n" \
"s_and_b32 %[tp], %[tp], 3\n" \
"s_and_b32 %[sX], %[u], 2147483644\n" \
"s_or_b32 %[tp], %[tp], %[sX]\n" \
"s_mul_i32 %[sX], %[tp], 4096\n" \
"s_mul_i32 %[sW], %[tp], 131072\n" \
"v_mfma_f32_16x16x32_bf16 v[28:31], v[92:95], v[44:47], v[28:31]\n" \
"s_waitcnt vmcnt(8) lgkmcnt(0)\n" \
"s_barrier\n" \
"s_mov_b32 %[cnt], 5\n" \
"LOOP_%=:\n" \
"v_mfma_f32_16x16x32_bf16 v[0:3], v[96:99], v[48:51], v[0:3]\n" \
"ds_read_b64_tr_b16 v[64:65], v131 offset:16384\n" \
"ds_read_b64_tr_b16 v[66:67], v131 offset:16896\n" \
"v_mfma_f32_16x16x32_bf16 v[4:7], v[96:99], v[52:55], v[4:7]\n" \
"ds_read_b128 v[32:35], v129 offset:0\n" \
"ds_read_b128 v[36:39], v129 offset:256\n" \
"v_mfma_f32_16x16x32_bf16 v[8:11], v[100:103], v[48:51], v[8:11]\n" \
"s_add_u32 m0, %[m0x], 32768\n" \
"s_nop 0\n" \
"buffer_load_dwordx4 v138, %[rX], %[sX] offen offset:0 sc1 lds\n" \
"ds_read_b64_tr_b16 v[68:69], v133 offset:16384\n" \
"v_mfma_f32_16x16x32_bf16 v[12:15], v[100:103], v[52:55], v[12:15]\n" \
"ds_read_b64_tr_b16 v[70:71], v133 offset:16896\n" \
"ds_read_b64_tr_b16 v[72:73], v135 offset:16384\n" \
"v_mfma_f32_16x16x32_bf16 v[16:19], v[104:107], v[48:51], v[16:19]\n" \
"ds_read_b64_tr_b16 v[74:75], v135 offset:16896\n" \
"ds_read_b64_tr_b16 v[76:77], v137 offset:16384\n" \
"v_mfma_f32_16x16x32_bf16 v[20:23], v[104:107], v[52:55], v[20:23]\n" \
"ds_read_b64_tr_b16 v[78:79], v137 offset:16896\n" \
"v_mfma_f32_16x16x32_bf16 v[24:27], v[108:111], v[48:51], v[24:27]\n" \
"ds_read_b64_tr_b16 v[80:81], v131 offset:20480\n" \
"buffer_load_dwordx4 v138, %[rX], %[sX] offen offset:1024 sc1 lds\n" \
"ds_read_b64_tr_b16 v[82:83], v131 offset:20992\n" \
"v_mfma_f32_16x16x32_bf16 v[28:31], v[108:111], v[52:55], v[28:31]\n" \
"ds_read_b128 v[40:43], v129 offset:2048\n" \
"ds_read_b128 v[44:47], v129 offset:2304\n" \
"v_mfma_f32_16x16x32_bf16 v[0:3], v[112:115], v[56:59], v[0:3]\n" \
"ds_read_b64_tr_b16 v[84:85], v133 offset:20480\n" \
"v_mfma_f32_16x16x32_bf16 v[4:7], v[112:115], v[60:63], v[4:7]\n" \
"ds_read_b64_tr_b16 v[86:87], v133 offset:20992\n" \
"ds_read_b64_tr_b16 v[88:89], v135 offset:20480\n" \
"v_mfma_f32_16x16x32_bf16 v[8:11], v[116:119], v[56:59], v[8:11]\n" \
"s_add_u32 m0, %[m0w], 49152\n" \
"s_nop 0\n" \
"buffer_load_dwordx4 v139, %[rW], %[sW] offen offset:0 lds\n" \
"ds_read_b64_tr_b16 v[90:91], v135 offset:20992\n" \
"ds_read_b64_tr_b16 v[92:93], v137 offset:20480\n" \
"v_mfma_f32_16x16x32_bf16 v[12:15], v[116:119], v[60:63], v[12:15]\n" \
"ds_read_b64_tr_b16 v[94:95], v137 offset:20992\n" \
"v_mfma_f32_16x16x32_bf16 v[16:19], v[120:123], v[56:59], v[16:19]\n" \
"v_mfma_f32_16x16x32_bf16 v[20:23], v[120:123], v[60:63], v[20:23]\n" \
"v_mfma_f32_16x16x32_bf16 v[24:27], v[124:127], v[56:59], v[24:27]\n" \
"buffer_load_dwordx4 v140, %[rW], %[sW] offen offset:1024 lds\n" \
"s_add_u32 %[u], %[u], 1\n" \
"s_add_u32 %[tp], %[u], %[rot]\n" \
"s_and_b32 %[tp], %[tp], 3\n" \
"s_and_b32 %[sX], %[u], 2147483644\n" \
"s_or_b32 %[tp], %[tp], %[sX]\n" \
"s_mul_i32 %[sX], %[tp], 4096\n" \
"s_mul_i32 %[sW], %[tp], 131072\n" \
"v_mfma_f32_16x16x32_bf16 v[28:31], v[124:127], v[60:63], v[28:31]\n" \
"s_waitcnt vmcnt(8) lgkmcnt(0)\n" \
"s_barrier\n" \
"v_mfma_f32_16x16x32_bf16 v[0:3], v[64:67], v[32:35], v[0:3]\n" \
"ds_read_b64_tr_b16 v[96:97], v131 offset:49152\n" \
"ds_read_b64_tr_b16 v[98:99], v131 offset:49664\n" \
"v_mfma_f32_16x16x32_bf16 v[4:7], v[64:67], v[36:39], v[4:7]\n" \
"ds_read_b128 v[48:51], v129 offset:32768\n" \
"ds_read_b128 v[52:55], v129 offset:33024\n" \
"v_mfma_f32_16x16x32_bf16 v[8:11], v[68:71], v[32:35], v[8:11]\n" \
"s_add_u32 m0, %[m0x], 65536\n" \
"s_nop 0\n" \
"buffer_load_dwordx4 v138, %[rX], %[sX] offen offset:0 sc1 lds\n" \
"ds_read_b64_tr_b16 v[100:101], v133 offset:49152\n" \
"v_mfma_f32_16x16x32_bf16 v[12:15], v[68:71], v[36:39], v[12:15]\n" \
"ds_read_b64_tr_b16 v[102:103], v133 offset:49664\n" \
"ds_read_b64_tr_b16 v[104:105], v135 offset:49152\n" \
"v_mfma_f32_16x16x32_bf16 v[16:19], v[72:75], v[32:35], v[16:19]\n" \
"ds_read_b64_tr_b16 v[106:107], v135 offset:49664\n" \
"ds_read_b64_tr_b16 v[108:109], v137 offset:49152\n" \
"v_mfma_f32_16x16x32_bf16 v[20:23], v[72:75], v[36:39], v[20:23]\n" \
"ds_read_b64_tr_b16 v[110:111], v137 offset:49664\n" \
"v_mfma_f32_16x16x32_bf16 v[24:27], v[76:79], v[32:35], v[24:27]\n" \
"ds_read_b64_tr_b16 v[112:113], v131 offset:53248\n" \
"buffer_load_dwordx4 v138, %[rX], %[sX] offen offset:1024 sc1 lds\n" \
"ds_read_b64_tr_b16 v[114:115], v131 offset:53760\n" \
"v_mfma_f32_16x16x32_bf16 v[28:31], v[76:79], v[36:39], v[28:31]\n" \
"ds_read_b128 v[56:59], v129 offset:34816\n" \
"ds_read_b128 v[60:63], v129 offset:35072\n" \
"v_mfma_f32_16x16x32_bf16 v[0:3], v[80:83], v[40:43], v[0:3]\n" \
"ds_read_b64_tr_b16 v[116:117], v133 offset:53248\n" \
"v_mfma_f32_16x16x32_bf16 v[4:7], v[80:83], v[44:47], v[4:7]\n" \
"ds_read_b64_tr_b16 v[118:119], v133 offset:53760\n" \
"ds_read_b64_tr_b16 v[120:121], v135 offset:53248\n" \
"v_mfma_f32_16x16x32_bf16 v[8:11], v[84:87], v[40:43], v[8:11]\n" \
"s_add_u32 m0, %[m0w], 81920\n" \
"s_nop 0\n" \
"buffer_load_dwordx4 v139, %[rW], %[sW] offen offset:0 lds\n" \
"ds_read_b64_tr_b16 v[122:123], v135 offset:53760\n" \
"ds_read_b64_tr_b16 v[124:125], v137 offset:53248\n" \
"v_mfma_f32_16x16x32_bf16 v[12:15], v[84:87], v[44:47], v[12:15]\n" \
"ds_read_b64_tr_b16 v[126:127], v137 offset:53760\n" \
"v_mfma_f32_16x16x32_bf16 v[16:19], v[88:91], v[40:43], v[16:19]\n" \
"v_mfma_f32_16x16x32_bf16 v[20:23], v[88:91], v[44:47], v[20:23]\n" \
"v_mfma_f32_16x16x32_bf16 v[24:27], v[92:95], v[40:43], v[24:27]\n" \
"buffer_load_dwordx4 v140, %[rW], %[sW] offen offset:1024 lds\n" \
"s_add_u32 %[u], %[u], 1\n" \
"s_add_u32 %[tp], %[u], %[rot]\n" \
"s_and_b32 %[tp], %[tp], 3\n" \
"s_and_b32 %[sX], %[u], 2147483644\n" \
"s_or_b32 %[tp], %[tp], %[sX]\n" \
"s_mul_i32 %[sX], %[tp], 4096\n" \
"s_mul_i32 %[sW], %[tp], 131072\n" \
"v_mfma_f32_16x16x32_bf16 v[28:31], v[92:95], v[44:47], v[28:31]\n" \
"s_waitcnt vmcnt(8) lgkmcnt(0)\n" \
"s_barrier\n" \
"v_mfma_f32_16x16x32_bf16 v[0:3], v[96:99], v[48:51], v[0:3]\n" \
"ds_read_b64_tr_b16 v[64:65], v130 offset:16384\n" \
"ds_read_b64_tr_b16 v[66:67], v130 offset:16896\n" \
"v_mfma_f32_16x16x32_bf16 v[4:7], v[96:99], v[52:55], v[4:7]\n" \
"ds_read_b128 v[32:35], v128 offset:0\n" \
"ds_read_b128 v[36:39], v128 offset:256\n" \
"v_mfma_f32_16x16x32_bf16 v[8:11], v[100:103], v[48:51], v[8:11]\n" \
"s_add_u32 m0, %[m0x], 98304\n" \
"s_nop 0\n" \
"buffer_load_dwordx4 v138, %[rX], %[sX] offen offset:0 sc1 lds\n" \
"ds_read_b64_tr_b16 v[68:69], v132 offset:16384\n" \
"v_mfma_f32_16x16x32_bf16 v[12:15], v[100:103], v[52:55], v[12:15]\n" \
"ds_read_b64_tr_b16 v[70:71], v132 offset:16896\n" \
"ds_read_b64_tr_b16 v[72:73], v134 offset:16384\n" \
"v_mfma_f32_16x16x32_bf16 v[16:19], v[104:107], v[48:51], v[16:19]\n" \
"ds_read_b64_tr_b16 v[74:75], v134 offset:16896\n" \
"ds_read_b64_tr_b16 v[76:77], v136 offset:16384\n" \
"v_mfma_f32_16x16x32_bf16 v[20:23], v[104:107], v[52:55], v[20:23]\n" \
"ds_read_b64_tr_b16 v[78:79], v136 offset:16896\n" \
"v_mfma_f32_16x16x32_bf16 v[24:27], v[108:111], v[48:51], v[24:27]\n" \
"ds_read_b64_tr_b16 v[80:81], v130 offset:20480\n" \
"buffer_load_dwordx4 v138, %[rX], %[sX] offen offset:1024 sc1 lds\n" \
"ds_read_b64_tr_b16 v[82:83], v130 offset:20992\n" \
"v_mfma_f32_16x16x32_bf16 v[28:31], v[108:111], v[52:55], v[28:31]\n" \
"ds_read_b128 v[40:43], v128 offset:2048\n" \
"ds_read_b128 v[44:47], v128 offset:2304\n" \
"v_mfma_f32_16x16x32_bf16 v[0:3], v[112:115], v[56:59], v[0:3]\n" \
"ds_read_b64_tr_b16 v[84:85], v132 offset:20480\n" \
"v_mfma_f32_16x16x32_bf16 v[4:7], v[112:115], v[60:63], v[4:7]\n" \
"ds_read_b64_tr_b16 v[86:87], v132 offset:20992\n" \
"ds_read_b64_tr_b16 v[88:89], v134 offset:20480\n" \
"v_mfma_f32_16x16x32_bf16 v[8:11], v[116:119], v[56:59], v[8:11]\n" \
"s_add_u32 m0, %[m0w], 114688\n" \
"s_nop 0\n" \
"buffer_load_dwordx4 v139, %[rW], %[sW] offen offset:0 lds\n" \
"ds_read_b64_tr_b16 v[90:91], v134 offset:20992\n" \
"ds_read_b64_tr_b16 v[92:93], v136 offset:20480\n" \
"v_mfma_f32_16x16x32_bf16 v[12:15], v[116:119], v[60:63], v[12:15]\n" \
"ds_read_b64_tr_b16 v[94:95], v136 offset:20992\n" \
"v_mfma_f32_16x16x32_bf16 v[16:19], v[120:123], v[56:59], v[16:19]\n" \
"v_mfma_f32_16x16x32_bf16 v[20:23], v[120:123], v[60:63], v[20:23]\n" \
"v_mfma_f32_16x16x32_bf16 v[24:27], v[124:127], v[56:59], v[24:27]\n" \
"buffer_load_dwordx4 v140, %[rW], %[sW] offen offset:1024 lds\n" \
"s_add_u32 %[u], %[u], 1\n" \
"s_add_u32 %[tp], %[u], %[rot]\n" \
"s_and_b32 %[tp], %[tp], 3\n" \
"s_and_b32 %[sX], %[u], 2147483644\n" \
"s_or_b32 %[tp], %[tp], %[sX]\n" \
"s_mul_i32 %[sX], %[tp], 4096\n" \
"s_mul_i32 %[sW], %[tp], 131072\n" \
"v_mfma_f32_16x16x32_bf16 v[28:31], v[124:127], v[60:63], v[28:31]\n" \
"s_waitcnt vmcnt(8) lgkmcnt(0)\n" \
"s_barrier\n" \
"v_mfma_f32_16x16x32_bf16 v[0:3], v[64:67], v[32:35], v[0:3]\n" \
"ds_read_b64_tr_b16 v[96:97], v130 offset:49152\n" \
"ds_read_b64_tr_b16 v[98:99], v130 offset:49664\n" \
"v_mfma_f32_16x16x32_bf16 v[4:7], v[64:67], v[36:39], v[4:7]\n" \
"ds_read_b128 v[48:51], v128 offset:32768\n" \
"ds_read_b128 v[52:55], v128 offset:33024\n" \
"v_mfma_f32_16x16x32_bf16 v[8:11], v[68:71], v[32:35], v[8:11]\n" \
"s_add_u32 m0, %[m0x], 0\n" \
"s_nop 0\n" \
"buffer_load_dwordx4 v138, %[rX], %[sX] offen offset:0 sc1 lds\n" \
"ds_read_b64_tr_b16 v[100:101], v132 offset:49152\n" \
"v_mfma_f32_16x16x32_bf16 v[12:15], v[68:71], v[36:39], v[12:15]\n" \
"ds_read_b64_tr_b16 v[102:103], v132 offset:49664\n" \
"ds_read_b64_tr_b16 v[104:105], v134 offset:49152\n" \
"v_mfma_f32_16x16x32_bf16 v[16:19], v[72:75], v[32:35], v[16:19]\n" \
"ds_read_b64_tr_b16 v[106:107], v134 offset:49664\n" \
"ds_read_b64_tr_b16 v[108:109], v136 offset:49152\n" \
"v_mfma_f32_16x16x32_bf16 v[20:23], v[72:75], v[36:39], v[20:23]\n" \
"ds_read_b64_tr_b16 v[110:111], v136 offset:49664\n" \
"v_mfma_f32_16x16x32_bf16 v[24:27], v[76:79], v[32:35], v[24:27]\n" \
"ds_read_b64_tr_b16 v[112:113], v130 offset:53248\n" \
"buffer_load_dwordx4 v138, %[rX], %[sX] offen offset:1024 sc1 lds\n" \
"ds_read_b64_tr_b16 v[114:115], v130 offset:53760\n" \
"v_mfma_f32_16x16x32_bf16 v[28:31], v[76:79], v[36:39], v[28:31]\n" \
"ds_read_b128 v[56:59], v128 offset:34816\n" \
"ds_read_b128 v[60:63], v128 offset:35072\n" \
"v_mfma_f32_16x16x32_bf16 v[0:3], v[80:83], v[40:43], v[0:3]\n" \
"ds_read_b64_tr_b16 v[116:117], v132 offset:53248\n" \
"v_mfma_f32_16x16x32_bf16 v[4:7], v[80:83], v[44:47], v[4:7]\n" \
"ds_read_b64_tr_b16 v[118:119], v132 offset:53760\n" \
"ds_read_b64_tr_b16 v[120:121], v134 offset:53248\n" \
"v_mfma_f32_16x16x32_bf16 v[8:11], v[84:87], v[40:43], v[8:11]\n" \
"s_add_u32 m0, %[m0w], 16384\n" \
"s_nop 0\n" \
"buffer_load_dwordx4 v139, %[rW], %[sW] offen offset:0 lds\n" \
"ds_read_b64_tr_b16 v[122:123], v134 offset:53760\n" \
"ds_read_b64_tr_b16 v[124:125], v136 offset:53248\n" \
"v_mfma_f32_16x16x32_bf16 v[12:15], v[84:87], v[44:47], v[12:15]\n" \
"ds_read_b64_tr_b16 v[126:127], v136 offset:53760\n" \
"v_mfma_f32_16x16x32_bf16 v[16:19], v[88:91], v[40:43], v[16:19]\n" \
"v_mfma_f32_16x16x32_bf16 v[20:23], v[88:91], v[44:47], v[20:23]\n" \
"v_mfma_f32_16x16x32_bf16 v[24:27], v[92:95], v[40:43], v[24:27]\n" \
"buffer_load_dwordx4 v140, %[rW], %[sW] offen offset:1024 lds\n" \
"s_add_u32 %[u], %[u], 1\n" \
"s_add_u32 %[tp], %[u], %[rot]\n" \
"s_and_b32 %[tp], %[tp], 3\n" \
"s_and_b32 %[sX], %[u], 2147483644\n" \
"s_or_b32 %[tp], %[tp], %[sX]\n" \
"s_mul_i32 %[sX], %[tp], 4096\n" \
"s_mul_i32 %[sW], %[tp], 131072\n" \
"v_mfma_f32_16x16x32_bf16 v[28:31], v[92:95], v[44:47], v[28:31]\n" \
"s_waitcnt vmcnt(8) lgkmcnt(0)\n" \
"s_barrier\n" \
"s_sub_u32 %[cnt], %[cnt], 1\n" \
"s_cmp_lg_u32 %[cnt], 0\n" \
"s_cbranch_scc1 LOOP_%=\n" \
"v_mfma_f32_16x16x32_bf16 v[0:3], v[96:99], v[48:51], v[0:3]\n" \
"ds_read_b64_tr_b16 v[64:65], v131 offset:16384\n" \
"ds_read_b64_tr_b16 v[66:67], v131 offset:16896\n" \
"v_mfma_f32_16x16x32_bf16 v[4:7], v[96:99], v[52:55], v[4:7]\n" \
"ds_read_b128 v[32:35], v129 offset:0\n" \
"ds_read_b128 v[36:39], v129 offset:256\n" \
"v_mfma_f32_16x16x32_bf16 v[8:11], v[100:103], v[48:51], v[8:11]\n" \
"s_add_u32 m0, %[m0x], 32768\n" \
"s_nop 0\n" \
"buffer_load_dwordx4 v138, %[rX], %[sX] offen offset:0 sc1 lds\n" \
"ds_read_b64_tr_b16 v[68:69], v133 offset:16384\n" \
"v_mfma_f32_16x16x32_bf16 v[12:15], v[100:103], v[52:55], v[12:15]\n" \
"ds_read_b64_tr_b16 v[70:71], v133 offset:16896\n" \
"ds_read_b64_tr_b16 v[72:73], v135 offset:16384\n" \
"v_mfma_f32_16x16x32_bf16 v[16:19], v[104:107], v[48:51], v[16:19]\n" \
"ds_read_b64_tr_b16 v[74:75], v135 offset:16896\n" \
"ds_read_b64_tr_b16 v[76:77], v137 offset:16384\n" \
"v_mfma_f32_16x16x32_bf16 v[20:23], v[104:107], v[52:55], v[20:23]\n" \
"ds_read_b64_tr_b16 v[78:79], v137 offset:16896\n" \
"v_mfma_f32_16x16x32_bf16 v[24:27], v[108:111], v[48:51], v[24:27]\n" \
"ds_read_b64_tr_b16 v[80:81], v131 offset:20480\n" \
"buffer_load_dwordx4 v138, %[rX], %[sX] offen offset:1024 sc1 lds\n" \
"ds_read_b64_tr_b16 v[82:83], v131 offset:20992\n" \
"v_mfma_f32_16x16x32_bf16 v[28:31], v[108:111], v[52:55], v[28:31]\n" \
"ds_read_b128 v[40:43], v129 offset:2048\n" \
"ds_read_b128 v[44:47], v129 offset:2304\n" \
"v_mfma_f32_16x16x32_bf16 v[0:3], v[112:115], v[56:59], v[0:3]\n" \
"ds_read_b64_tr_b16 v[84:85], v133 offset:20480\n" \
"v_mfma_f32_16x16x32_bf16 v[4:7], v[112:115], v[60:63], v[4:7]\n" \
"ds_read_b64_tr_b16 v[86:87], v133 offset:20992\n" \
"ds_read_b64_tr_b16 v[88:89], v135 offset:20480\n" \
"v_mfma_f32_16x16x32_bf16 v[8:11], v[116:119], v[56:59], v[8:11]\n" \
"s_add_u32 m0, %[m0w], 49152\n" \
"s_nop 0\n" \
"buffer_load_dwordx4 v139, %[rW], %[sW] offen offset:0 lds\n" \
"ds_read_b64_tr_b16 v[90:91], v135 offset:20992\n" \
"ds_read_b64_tr_b16 v[92:93], v137 offset:20480\n" \
"v_mfma_f32_16x16x32_bf16 v[12:15], v[116:119], v[60:63], v[12:15]\n" \
"ds_read_b64_tr_b16 v[94:95], v137 offset:20992\n" \
"v_mfma_f32_16x16x32_bf16 v[16:19], v[120:123], v[56:59], v[16:19]\n" \
"v_mfma_f32_16x16x32_bf16 v[20:23], v[120:123], v[60:63], v[20:23]\n" \
"v_mfma_f32_16x16x32_bf16 v[24:27], v[124:127], v[56:59], v[24:27]\n" \
"buffer_load_dwordx4 v140, %[rW], %[sW] offen offset:1024 lds\n" \
"s_add_u32 %[u], %[u], 1\n" \
"s_add_u32 %[tp], %[u], %[rot]\n" \
"s_and_b32 %[tp], %[tp], 3\n" \
"s_and_b32 %[sX], %[u], 2147483644\n" \
"s_or_b32 %[tp], %[tp], %[sX]\n" \
"s_mul_i32 %[sX], %[tp], 4096\n" \
"s_mul_i32 %[sW], %[tp], 131072\n" \
"v_mfma_f32_16x16x32_bf16 v[28:31], v[124:127], v[60:63], v[28:31]\n" \
"s_waitcnt vmcnt(8) lgkmcnt(0)\n" \
"s_barrier\n" \
"v_mfma_f32_16x16x32_bf16 v[0:3], v[64:67], v[32:35], v[0:3]\n" \
"ds_read_b64_tr_b16 v[96:97], v131 offset:49152\n" \
"ds_read_b64_tr_b16 v[98:99], v131 offset:49664\n" \
"v_mfma_f32_16x16x32_bf16 v[4:7], v[64:67], v[36:39], v[4:7]\n" \
"ds_read_b128 v[48:51], v129 offset:32768\n" \
"ds_read_b128 v[52:55], v129 offset:33024\n" \
"v_mfma_f32_16x16x32_bf16 v[8:11], v[68:71], v[32:35], v[8:11]\n" \
"s_add_u32 m0, %[m0x], 65536\n" \
"s_nop 0\n" \
"buffer_load_dwordx4 v138, %[rX], %[sX] offen offset:0 sc1 lds\n" \
"ds_read_b64_tr_b16 v[100:101], v133 offset:49152\n" \
"v_mfma_f32_16x16x32_bf16 v[12:15], v[68:71], v[36:39], v[12:15]\n" \
"ds_read_b64_tr_b16 v[102:103], v133 offset:49664\n" \
"ds_read_b64_tr_b16 v[104:105], v135 offset:49152\n" \
"v_mfma_f32_16x16x32_bf16 v[16:19], v[72:75], v[32:35], v[16:19]\n" \
"ds_read_b64_tr_b16 v[106:107], v135 offset:49664\n" \
"ds_read_b64_tr_b16 v[108:109], v137 offset:49152\n" \
"v_mfma_f32_16x16x32_bf16 v[20:23], v[72:75], v[36:39], v[20:23]\n" \
"ds_read_b64_tr_b16 v[110:111], v137 offset:49664\n" \
"v_mfma_f32_16x16x32_bf16 v[24:27], v[76:79], v[32:35], v[24:27]\n" \
"ds_read_b64_tr_b16 v[112:113], v131 offset:53248\n" \
"buffer_load_dwordx4 v138, %[rX], %[sX] offen offset:1024 sc1 lds\n" \
"ds_read_b64_tr_b16 v[114:115], v131 offset:53760\n" \
"v_mfma_f32_16x16x32_bf16 v[28:31], v[76:79], v[36:39], v[28:31]\n" \
"ds_read_b128 v[56:59], v129 offset:34816\n" \
"ds_read_b128 v[60:63], v129 offset:35072\n" \
"v_mfma_f32_16x16x32_bf16 v[0:3], v[80:83], v[40:43], v[0:3]\n" \
"ds_read_b64_tr_b16 v[116:117], v133 offset:53248\n" \
"v_mfma_f32_16x16x32_bf16 v[4:7], v[80:83], v[44:47], v[4:7]\n" \
"ds_read_b64_tr_b16 v[118:119], v133 offset:53760\n" \
"ds_read_b64_tr_b16 v[120:121], v135 offset:53248\n" \
"v_mfma_f32_16x16x32_bf16 v[8:11], v[84:87], v[40:43], v[8:11]\n" \
"s_add_u32 m0, %[m0w], 81920\n" \
"s_nop 0\n" \
"buffer_load_dwordx4 v139, %[rW], %[sW] offen offset:0 lds\n" \
"ds_read_b64_tr_b16 v[122:123], v135 offset:53760\n" \
"ds_read_b64_tr_b16 v[124:125], v137 offset:53248\n" \
"v_mfma_f32_16x16x32_bf16 v[12:15], v[84:87], v[44:47], v[12:15]\n" \
"ds_read_b64_tr_b16 v[126:127], v137 offset:53760\n" \
"v_mfma_f32_16x16x32_bf16 v[16:19], v[88:91], v[40:43], v[16:19]\n" \
"v_mfma_f32_16x16x32_bf16 v[20:23], v[88:91], v[44:47], v[20:23]\n" \
"v_mfma_f32_16x16x32_bf16 v[24:27], v[92:95], v[40:43], v[24:27]\n" \
"buffer_load_dwordx4 v140, %[rW], %[sW] offen offset:1024 lds\n" \
"s_add_u32 %[u], %[u], 1\n" \
"s_add_u32 %[tp], %[u], %[rot]\n" \
"s_and_b32 %[tp], %[tp], 3\n" \
"s_and_b32 %[sX], %[u], 2147483644\n" \
"s_or_b32 %[tp], %[tp], %[sX]\n" \
"s_mul_i32 %[sX], %[tp], 4096\n" \
"s_mul_i32 %[sW], %[tp], 131072\n" \
"v_mfma_f32_16x16x32_bf16 v[28:31], v[92:95], v[44:47], v[28:31]\n" \
"s_waitcnt vmcnt(8) lgkmcnt(0)\n" \
"s_barrier\n" \
"v_mfma_f32_16x16x32_bf16 v[0:3], v[96:99], v[48:51], v[0:3]\n" \
"ds_read_b64_tr_b16 v[64:65], v130 offset:16384\n" \
"ds_read_b64_tr_b16 v[66:67], v130 offset:16896\n" \
"v_mfma_f32_16x16x32_bf16 v[4:7], v[96:99], v[52:55], v[4:7]\n" \
"ds_read_b128 v[32:35], v128 offset:0\n" \
"ds_read_b128 v[36:39], v128 offset:256\n" \
"v_mfma_f32_16x16x32_bf16 v[8:11], v[100:103], v[48:51], v[8:11]\n" \
"s_add_u32 m0, %[m0x], 98304\n" \
"s_nop 0\n" \
"buffer_load_dwordx4 v138, %[rX], %[sX] offen offset:0 sc1 lds\n" \
"ds_read_b64_tr_b16 v[68:69], v132 offset:16384\n" \
"v_mfma_f32_16x16x32_bf16 v[12:15], v[100:103], v[52:55], v[12:15]\n" \
"ds_read_b64_tr_b16 v[70:71], v132 offset:16896\n" \
"ds_read_b64_tr_b16 v[72:73], v134 offset:16384\n" \
"v_mfma_f32_16x16x32_bf16 v[16:19], v[104:107], v[48:51], v[16:19]\n" \
"ds_read_b64_tr_b16 v[74:75], v134 offset:16896\n" \
"ds_read_b64_tr_b16 v[76:77], v136 offset:16384\n" \
"v_mfma_f32_16x16x32_bf16 v[20:23], v[104:107], v[52:55], v[20:23]\n" \
"ds_read_b64_tr_b16 v[78:79], v136 offset:16896\n" \
"v_mfma_f32_16x16x32_bf16 v[24:27], v[108:111], v[48:51], v[24:27]\n" \
"ds_read_b64_tr_b16 v[80:81], v130 offset:20480\n" \
"buffer_load_dwordx4 v138, %[rX], %[sX] offen offset:1024 sc1 lds\n" \
"ds_read_b64_tr_b16 v[82:83], v130 offset:20992\n" \
"v_mfma_f32_16x16x32_bf16 v[28:31], v[108:111], v[52:55], v[28:31]\n" \
"ds_read_b128 v[40:43], v128 offset:2048\n" \
"ds_read_b128 v[44:47], v128 offset:2304\n" \
"v_mfma_f32_16x16x32_bf16 v[0:3], v[112:115], v[56:59], v[0:3]\n" \
"ds_read_b64_tr_b16 v[84:85], v132 offset:20480\n" \
"v_mfma_f32_16x16x32_bf16 v[4:7], v[112:115], v[60:63], v[4:7]\n" \
"ds_read_b64_tr_b16 v[86:87], v132 offset:20992\n" \
"ds_read_b64_tr_b16 v[88:89], v134 offset:20480\n" \
"v_mfma_f32_16x16x32_bf16 v[8:11], v[116:119], v[56:59], v[8:11]\n" \
"s_add_u32 m0, %[m0w], 114688\n" \
"s_nop 0\n" \
"buffer_load_dwordx4 v139, %[rW], %[sW] offen offset:0 lds\n" \
"ds_read_b64_tr_b16 v[90:91], v134 offset:20992\n" \
"ds_read_b64_tr_b16 v[92:93], v136 offset:20480\n" \
"v_mfma_f32_16x16x32_bf16 v[12:15], v[116:119], v[60:63], v[12:15]\n" \
"ds_read_b64_tr_b16 v[94:95], v136 offset:20992\n" \
"v_mfma_f32_16x16x32_bf16 v[16:19], v[120:123], v[56:59], v[16:19]\n" \
"v_mfma_f32_16x16x32_bf16 v[20:23], v[120:123], v[60:63], v[20:23]\n" \
"v_mfma_f32_16x16x32_bf16 v[24:27], v[124:127], v[56:59], v[24:27]\n" \
"buffer_load_dwordx4 v140, %[rW], %[sW] offen offset:1024 lds\n" \
"s_add_u32 %[u], %[u], 1\n" \
"s_add_u32 %[tp], %[u], %[rot]\n" \
"s_and_b32 %[tp], %[tp], 3\n" \
"s_and_b32 %[sX], %[u], 2147483644\n" \
"s_or_b32 %[tp], %[tp], %[sX]\n" \
"s_mul_i32 %[sX], %[tp], 4096\n" \
"s_mul_i32 %[sW], %[tp], 131072\n" \
"v_mfma_f32_16x16x32_bf16 v[28:31], v[124:127], v[60:63], v[28:31]\n" \
"s_waitcnt vmcnt(8) lgkmcnt(0)\n" \
"s_barrier\n" \
"v_mfma_f32_16x16x32_bf16 v[0:3], v[64:67], v[32:35], v[0:3]\n" \
"ds_read_b64_tr_b16 v[96:97], v130 offset:49152\n" \
"ds_read_b64_tr_b16 v[98:99], v130 offset:49664\n" \
"v_mfma_f32_16x16x32_bf16 v[4:7], v[64:67], v[36:39], v[4:7]\n" \
"ds_read_b128 v[48:51], v128 offset:32768\n" \
"ds_read_b128 v[52:55], v128 offset:33024\n" \
"v_mfma_f32_16x16x32_bf16 v[8:11], v[68:71], v[32:35], v[8:11]\n" \
"ds_read_b64_tr_b16 v[100:101], v132 offset:49152\n" \
"v_mfma_f32_16x16x32_bf16 v[12:15], v[68:71], v[36:39], v[12:15]\n" \
"ds_read_b64_tr_b16 v[102:103], v132 offset:49664\n" \
"ds_read_b64_tr_b16 v[104:105], v134 offset:49152\n" \
"v_mfma_f32_16x16x32_bf16 v[16:19], v[72:75], v[32:35], v[16:19]\n" \
"ds_read_b64_tr_b16 v[106:107], v134 offset:49664\n" \
"ds_read_b64_tr_b16 v[108:109], v136 offset:49152\n" \
"v_mfma_f32_16x16x32_bf16 v[20:23], v[72:75], v[36:39], v[20:23]\n" \
"ds_read_b64_tr_b16 v[110:111], v136 offset:49664\n" \
"v_mfma_f32_16x16x32_bf16 v[24:27], v[76:79], v[32:35], v[24:27]\n" \
"ds_read_b64_tr_b16 v[112:113], v130 offset:53248\n" \
"ds_read_b64_tr_b16 v[114:115], v130 offset:53760\n" \
"v_mfma_f32_16x16x32_bf16 v[28:31], v[76:79], v[36:39], v[28:31]\n" \
"ds_read_b128 v[56:59], v128 offset:34816\n" \
"ds_read_b128 v[60:63], v128 offset:35072\n" \
"v_mfma_f32_16x16x32_bf16 v[0:3], v[80:83], v[40:43], v[0:3]\n" \
"ds_read_b64_tr_b16 v[116:117], v132 offset:53248\n" \
"v_mfma_f32_16x16x32_bf16 v[4:7], v[80:83], v[44:47], v[4:7]\n" \
"ds_read_b64_tr_b16 v[118:119], v132 offset:53760\n" \
"ds_read_b64_tr_b16 v[120:121], v134 offset:53248\n" \
"v_mfma_f32_16x16x32_bf16 v[8:11], v[84:87], v[40:43], v[8:11]\n" \
"ds_read_b64_tr_b16 v[122:123], v134 offset:53760\n" \
"ds_read_b64_tr_b16 v[124:125], v136 offset:53248\n" \
"v_mfma_f32_16x16x32_bf16 v[12:15], v[84:87], v[44:47], v[12:15]\n" \
"ds_read_b64_tr_b16 v[126:127], v136 offset:53760\n" \
"v_mfma_f32_16x16x32_bf16 v[16:19], v[88:91], v[40:43], v[16:19]\n" \
"v_mfma_f32_16x16x32_bf16 v[20:23], v[88:91], v[44:47], v[20:23]\n" \
"v_mfma_f32_16x16x32_bf16 v[24:27], v[92:95], v[40:43], v[24:27]\n" \
"v_mfma_f32_16x16x32_bf16 v[28:31], v[92:95], v[44:47], v[28:31]\n" \
"s_waitcnt vmcnt(4) lgkmcnt(0)\n" \
"s_barrier\n" \
"v_mfma_f32_16x16x32_bf16 v[0:3], v[96:99], v[48:51], v[0:3]\n" \
"ds_read_b64_tr_b16 v[64:65], v131 offset:16384\n" \
"ds_read_b64_tr_b16 v[66:67], v131 offset:16896\n" \
"v_mfma_f32_16x16x32_bf16 v[4:7], v[96:99], v[52:55], v[4:7]\n" \
"ds_read_b128 v[32:35], v129 offset:0\n" \
"ds_read_b128 v[36:39], v129 offset:256\n" \
"v_mfma_f32_16x16x32_bf16 v[8:11], v[100:103], v[48:51], v[8:11]\n" \
"ds_read_b64_tr_b16 v[68:69], v133 offset:16384\n" \
"v_mfma_f32_16x16x32_bf16 v[12:15], v[100:103], v[52:55], v[12:15]\n" \
"ds_read_b64_tr_b16 v[70:71], v133 offset:16896\n" \
"ds_read_b64_tr_b16 v[72:73], v135 offset:16384\n" \
"v_mfma_f32_16x16x32_bf16 v[16:19], v[104:107], v[48:51], v[16:19]\n" \
"ds_read_b64_tr_b16 v[74:75], v135 offset:16896\n" \
"ds_read_b64_tr_b16 v[76:77], v137 offset:16384\n" \
"v_mfma_f32_16x16x32_bf16 v[20:23], v[104:107], v[52:55], v[20:23]\n" \
"ds_read_b64_tr_b16 v[78:79], v137 offset:16896\n" \
"v_mfma_f32_16x16x32_bf16 v[24:27], v[108:111], v[48:51], v[24:27]\n" \
"ds_read_b64_tr_b16 v[80:81], v131 offset:20480\n" \
"ds_read_b64_tr_b16 v[82:83], v131 offset:20992\n" \
"v_mfma_f32_16x16x32_bf16 v[28:31], v[108:111], v[52:55], v[28:31]\n" \
"ds_read_b128 v[40:43], v129 offset:2048\n" \
"ds_read_b128 v[44:47], v129 offset:2304\n" \
"v_mfma_f32_16x16x32_bf16 v[0:3], v[112:115], v[56:59], v[0:3]\n" \
"ds_read_b64_tr_b16 v[84:85], v133 offset:20480\n" \
"v_mfma_f32_16x16x32_bf16 v[4:7], v[112:115], v[60:63], v[4:7]\n" \
"ds_read_b64_tr_b16 v[86:87], v133 offset:20992\n" \
"ds_read_b64_tr_b16 v[88:89], v135 offset:20480\n" \
"v_mfma_f32_16x16x32_bf16 v[8:11], v[116:119], v[56:59], v[8:11]\n" \
"ds_read_b64_tr_b16 v[90:91], v135 offset:20992\n" \
"ds_read_b64_tr_b16 v[92:93], v137 offset:20480\n" \
"v_mfma_f32_16x16x32_bf16 v[12:15], v[116:119], v[60:63], v[12:15]\n" \
"ds_read_b64_tr_b16 v[94:95], v137 offset:20992\n" \
"v_mfma_f32_16x16x32_bf16 v[16:19], v[120:123], v[56:59], v[16:19]\n" \
"v_mfma_f32_16x16x32_bf16 v[20:23], v[120:123], v[60:63], v[20:23]\n" \
"v_mfma_f32_16x16x32_bf16 v[24:27], v[124:127], v[56:59], v[24:27]\n" \
"v_mfma_f32_16x16x32_bf16 v[28:31], v[124:127], v[60:63], v[28:31]\n" \
"s_waitcnt vmcnt(0) lgkmcnt(0)\n" \
"s_barrier\n" \
"v_mfma_f32_16x16x32_bf16 v[0:3], v[64:67], v[32:35], v[0:3]\n" \
"ds_read_b64_tr_b16 v[96:97], v131 offset:49152\n" \
"ds_read_b64_tr_b16 v[98:99], v131 offset:49664\n" \
"v_mfma_f32_16x16x32_bf16 v[4:7], v[64:67], v[36:39], v[4:7]\n" \
"ds_read_b128 v[48:51], v129 offset:32768\n" \
"ds_read_b128 v[52:55], v129 offset:33024\n" \
"v_mfma_f32_16x16x32_bf16 v[8:11], v[68:71], v[32:35], v[8:11]\n" \
"ds_read_b64_tr_b16 v[100:101], v133 offset:49152\n" \
"v_mfma_f32_16x16x32_bf16 v[12:15], v[68:71], v[36:39], v[12:15]\n" \
"ds_read_b64_tr_b16 v[102:103], v133 offset:49664\n" \
"ds_read_b64_tr_b16 v[104:105], v135 offset:49152\n" \
"v_mfma_f32_16x16x32_bf16 v[16:19], v[72:75], v[32:35], v[16:19]\n" \
"ds_read_b64_tr_b16 v[106:107], v135 offset:49664\n" \
"ds_read_b64_tr_b16 v[108:109], v137 offset:49152\n" \
"v_mfma_f32_16x16x32_bf16 v[20:23], v[72:75], v[36:39], v[20:23]\n" \
"ds_read_b64_tr_b16 v[110:111], v137 offset:49664\n" \
"v_mfma_f32_16x16x32_bf16 v[24:27], v[76:79], v[32:35], v[24:27]\n" \
"ds_read_b64_tr_b16 v[112:113], v131 offset:53248\n" \
"ds_read_b64_tr_b16 v[114:115], v131 offset:53760\n" \
"v_mfma_f32_16x16x32_bf16 v[28:31], v[76:79], v[36:39], v[28:31]\n" \
"ds_read_b128 v[56:59], v129 offset:34816\n" \
"ds_read_b128 v[60:63], v129 offset:35072\n" \
"v_mfma_f32_16x16x32_bf16 v[0:3], v[80:83], v[40:43], v[0:3]\n" \
"ds_read_b64_tr_b16 v[116:117], v133 offset:53248\n" \
"v_mfma_f32_16x16x32_bf16 v[4:7], v[80:83], v[44:47], v[4:7]\n" \
"ds_read_b64_tr_b16 v[118:119], v133 offset:53760\n" \
"ds_read_b64_tr_b16 v[120:121], v135 offset:53248\n" \
"v_mfma_f32_16x16x32_bf16 v[8:11], v[84:87], v[40:43], v[8:11]\n" \
"ds_read_b64_tr_b16 v[122:123], v135 offset:53760\n" \
"ds_read_b64_tr_b16 v[124:125], v137 offset:53248\n" \
"v_mfma_f32_16x16x32_bf16 v[12:15], v[84:87], v[44:47], v[12:15]\n" \
"ds_read_b64_tr_b16 v[126:127], v137 offset:53760\n" \
"v_mfma_f32_16x16x32_bf16 v[16:19], v[88:91], v[40:43], v[16:19]\n" \
"v_mfma_f32_16x16x32_bf16 v[20:23], v[88:91], v[44:47], v[20:23]\n" \
"v_mfma_f32_16x16x32_bf16 v[24:27], v[92:95], v[40:43], v[24:27]\n" \
"v_mfma_f32_16x16x32_bf16 v[28:31], v[92:95], v[44:47], v[28:31]\n" \
"s_waitcnt lgkmcnt(15)\n" \
"v_mfma_f32_16x16x32_bf16 v[0:3], v[96:99], v[48:51], v[0:3]\n" \
"v_mfma_f32_16x16x32_bf16 v[4:7], v[96:99], v[52:55], v[4:7]\n" \
"s_waitcnt lgkmcnt(14)\n" \
"v_mfma_f32_16x16x32_bf16 v[8:11], v[100:103], v[48:51], v[8:11]\n" \
"v_mfma_f32_16x16x32_bf16 v[12:15], v[100:103], v[52:55], v[12:15]\n" \
"s_waitcnt lgkmcnt(12)\n" \
"v_mfma_f32_16x16x32_bf16 v[16:19], v[104:107], v[48:51], v[16:19]\n" \
"v_mfma_f32_16x16x32_bf16 v[20:23], v[104:107], v[52:55], v[20:23]\n" \
"s_waitcnt lgkmcnt(10)\n" \
"v_mfma_f32_16x16x32_bf16 v[24:27], v[108:111], v[48:51], v[24:27]\n" \
"v_mfma_f32_16x16x32_bf16 v[28:31], v[108:111], v[52:55], v[28:31]\n" \
"s_waitcnt lgkmcnt(7)\n" \
"v_mfma_f32_16x16x32_bf16 v[0:3], v[112:115], v[56:59], v[0:3]\n" \
"s_waitcnt lgkmcnt(6)\n" \
"v_mfma_f32_16x16x32_bf16 v[4:7], v[112:115], v[60:63], v[4:7]\n" \
"s_waitcnt lgkmcnt(4)\n" \
"v_mfma_f32_16x16x32_bf16 v[8:11], v[116:119], v[56:59], v[8:11]\n" \
"v_mfma_f32_16x16x32_bf16 v[12:15], v[116:119], v[60:63], v[12:15]\n" \
"s_waitcnt lgkmcnt(2)\n" \
"v_mfma_f32_16x16x32_bf16 v[16:19], v[120:123], v[56:59], v[16:19]\n" \
"v_mfma_f32_16x16x32_bf16 v[20:23], v[120:123], v[60:63], v[20:23]\n" \
"s_waitcnt lgkmcnt(0)\n" \
"v_mfma_f32_16x16x32_bf16 v[24:27], v[124:127], v[56:59], v[24:27]\n" \
"v_mfma_f32_16x16x32_bf16 v[28:31], v[124:127], v[60:63], v[28:31]\n" \
"s_nop 15\n" \
"s_nop 7\n" \
    : "={v[0:15]}"(acc[0]), "={v[16:31]}"(acc[1]), [sX] "+&s"(sX_), [sW] "+&s"(sW_), [cnt] "=&s"(cnt_), [u] "+&s"(u_), [tp] "=&s"(tp_) : "{v138}"(vxd[0]), "{v139}"(vwd[0]), "{v140}"(vwd[1]), "{v128}"(vxr[0] + 0u), "{v129}"(vxr[0] + 65536u), "{v130}"(vwr[0] + 0u), "{v131}"(vwr[0] + 65536u), "{v132}"(vwr[1] + 0u), "{v133}"(vwr[1] + 65536u), "{v134}"(vwr[2] + 0u), "{v135}"(vwr[2] + 65536u), "{v136}"(vwr[3] + 0u), "{v137}"(vwr[3] + 65536u), [rX] "s"(rX_), [rW] "s"(rW_), [m0x] "s"(m0x_), [m0w] "s"(m0w_), [rot] "s"(rot_) : "memory", "scc", "v32", "v33", "v34", "v35", "v36", "v37", "v38", "v39", "v40", "v41", "v42", "v43", "v44", "v45", "v46", "v47", "v64", "v65", "v66", "v67", "v68", "v69", "v70", "v71", "v72", "v73", "v74", "v75", "v76", "v77", "v78", "v79", "v80", "v81", "v82", "v83", "v84", "v85", "v86", "v87", "v88", "v89", "v90", "v91", "v92", "v93", "v94", "v95", "v48", "v49", "v50", "v51", "v52", "v53", "v54", "v55", "v56", "v57", "v58", "v59", "v60", "v61", "v62", "v63", "v96", "v97", "v98", "v99", "v100", "v101", "v102", "v103", "v104", "v105", "v106", "v107", "v108", "v109", "v110", "v111", "v112", "v113", "v114", "v115", "v116", "v117", "v118", "v119", "v120", "v121", "v122", "v123", "v124", "v125", "v126", "v127")


typedef unsigned short bf16_t;
typedef float f32x16 __attribute__((ext_vector_type(16)));
typedef float f32x4 __attribute__((ext_vector_type(4)));
typedef float f32x2 __attribute__((ext_vector_type(2)));
typedef unsigned u32x4 __attribute__((ext_vector_type(4)));
typedef unsigned u32x2 __attribute__((ext_vector_type(2)));
typedef __bf16 bf16x2 __attribute__((ext_vector_type(2)));
typedef __bf16 bf16x8 __attribute__((ext_vector_type(8)));
#define LAS __attribute__((address_space(3)))
#define DI __device__ __forceinline__

static constexpr int S_ = 2048, D_ = 1024, F_ = 3584;
static constexpr int NST = 16;

DI unsigned cvtpk(float lo, float hi) { f32x2 v = {lo, hi}; bf16x2 b = __builtin_convertvector(v, bf16x2); return __builtin_bit_cast(unsigned, b); }
DI float bflo(unsigned u) { return __uint_as_float(u << 16); }
DI float bfhi(unsigned u) { return __uint_as_float(u & 0xffff0000u); }
DI f32x4 fr4(const f32x16& v, int i) { return (f32x4){v[4 * i], v[4 * i + 1], v[4 * i + 2], v[4 * i + 3]}; }
DI f32x4 mfma16(u32x4 a, u32x4 b, f32x4 c) { return __builtin_amdgcn_mfma_f32_16x16x32_bf16(__builtin_bit_cast(bf16x8, a), __builtin_bit_cast(bf16x8, b), c, 0, 0, 0); }
DI __amdgpu_buffer_rsrc_t mkrsrc(const void* p, unsigned bytes) { return __builtin_amdgcn_make_buffer_rsrc((void*)p, 0, bytes, 0x00020000); }

#define TOUCH_T1 1400ull
#define TOUCH_T2 1700ull
#define PRIO_MODE 4
#define PRIO_ON(w, ph) do { if ((PRIO_MODE & (ph)) && (w) < 4) __builtin_amdgcn_s_setprio(1); } while (0)
#define PRIO_OFF(ph) do { if (PRIO_MODE & (ph)) __builtin_amdgcn_s_setprio(0); } while (0)
#define FIN_AUX 2
#define T_AWAIT 1
#define T_TAIL 1
#define ST_AUX 16
DI void st16(__amdgpu_buffer_rsrc_t r, unsigned off, u32x4 v) { __builtin_amdgcn_raw_buffer_store_b128(v, r, off, 0, ST_AUX); }

DI void l2_touch(__amdgpu_buffer_rsrc_t r, unsigned voff, unsigned lds_scratch) {
    asm volatile("s_mov_b32 m0, %2\n\ts_nop 0\n\tbuffer_load_dword %0, %1, 0 offen lds" :: "v"(voff), "s"(r), "s"(lds_scratch) : "memory");
}

template <int CTRL> DI float dpp_add(float x) { return x + __uint_as_float(__builtin_amdgcn_update_dpp(0u, __float_as_uint(x), CTRL, 0xf, 0xf, true)); }
DI float sum_xor1(float x) { return dpp_add<0xB1>(x); }
DI float sum_xor2(float x) { return dpp_add<0x4E>(x); }
DI float sum_half_mirror(float x) { return dpp_add<0x141>(x); }
DI float sum_ror4(float x) { return dpp_add<0x124>(x); }
DI float sum_ror8(float x) { return dpp_add<0x128>(x); }
DI float sum_swap16(float x) { const auto t = __builtin_amdgcn_permlane16_swap(__float_as_uint(x), __float_as_uint(x), false, false); return __uint_as_float(t[0]) + __uint_as_float(t[1]); }
DI float sum_swap32(float x) { const auto t = __builtin_amdgcn_permlane32_swap(__float_as_uint(x), __float_as_uint(x), false, false); return __uint_as_float(t[0]) + __uint_as_float(t[1]); }

template <class C> struct AuxT { f32x4 p0, p1, p2, p3; C c; };
typedef AuxT<float> Aux; typedef AuxT<f32x4> Aux4;
#define AUX_LOAD_(a, stp, row, half, csp, CI, CR) asm volatile( \
    "global_load_dwordx4 v[236:239], %5, %6 offset:0 sc1\n\tglobal_load_dwordx4 v[240:243], %5, %6 offset:16 sc1\n\t" \
    "global_load_dwordx4 v[244:247], %5, %6 offset:32 sc1\n\tglobal_load_dwordx4 v[248:251], %5, %6 offset:48 sc1\n\t" \
    CI " " CR ", %7, off sc1" \
    : "={v[236:239]}"(a.p0), "={v[240:243]}"(a.p1), "={v[244:247]}"(a.p2), "={v[248:251]}"(a.p3), "={" CR "}"(a.c) \
    : "v"((unsigned)(((row) * NST + (half) * (NST / 2)) * 8)), "s"(stp), "v"(csp) : "memory")
#define AUX_WAIT_(a, N, CR) asm volatile("s_waitcnt vmcnt(" #N ")" \
    : "+{v[236:239]}"(a.p0), "+{v[240:243]}"(a.p1), "+{v[244:247]}"(a.p2), "+{v[248:251]}"(a.p3), "+{" CR "}"(a.c) :: "memory")
#define AUX_LOAD(a, stp, row, half, csp) AUX_LOAD_(a, stp, row, half, csp, "global_load_dword", "v252")
#define AUX_WAIT(a, N) AUX_WAIT_(a, N, "v252")
#define AUX4_LOAD(a, stp, row, half, csp) AUX_LOAD_(a, stp, row, half, csp, "global_load_dwordx4", "v[252:255]")
#define AUX4_WAIT(a, N) AUX_WAIT_(a, N, "v[252:255]")
template <class A>
DI f32x2 aux_stats(const A& a) {
    float ms = ((a.p0[0] + a.p0[2]) + (a.p1[0] + a.p1[2])) + ((a.p2[0] + a.p2[2]) + (a.p3[0] + a.p3[2]));
    ms = sum_xor1(ms);
    const float mean = ms * (1.0f / NST);
    const f32x4 m4 = {mean, 0.f, mean, 0.f};
    const f32x4 d0 = a.p0 - m4, d1 = a.p1 - m4, d2 = a.p2 - m4, d3 = a.p3 - m4;
    float q = ((d0[1] + d0[3]) + (d1[1] + d1[3])) + ((d2[1] + d2[3]) + (d3[1] + d3[3]));
    q += 64.0f * (((d0[0] * d0[0] + d0[2] * d0[2]) + (d1[0] * d1[0] + d1[2] * d1[2])) + ((d2[0] * d2[0] + d2[2] * d2[2]) + (d3[0] * d3[0] + d3[2] * d3[2])));
    q = sum_xor1(q);
    return (f32x2){mean, __builtin_amdgcn_rsqf(q * (1.0f / D_) + 1e-5f)};
}

__global__ __launch_bounds__(256) void k_prep(const bf16_t* __restrict__ x, const bf16_t* __restrict__ wqkv, bf16_t* __restrict__ xt, f32x2* __restrict__ st, float* __restrict__ cs, unsigned* __restrict__ cnt) {
    __shared__ float red[4][64];
    const int b = blockIdx.x, tid = threadIdx.x, lane = tid & 63, w = tid >> 6, r = lane >> 3, c = lane & 7;
    const int row = 8 * b + r;
    u32x4 v[4], u[8];
#pragma unroll
    for (int t = 0; t < 4; t++) v[t] = *(const u32x4*)(x + (size_t)row * D_ + 64 * (4 * w + t) + 8 * c);
    const int g = (b & 7) >> 1, pc = (b & 1) * 24 + (b >> 3), strip = pc >> 2, kq = pc & 3, col0 = (strip >> 2) * D_ + 256 * g + 64 * (strip & 3);
    const bool has_piece = b < 192;
    if (has_piece) {
        const bf16_t* q = wqkv + (size_t)(256 * kq + 64 * w + r) * (3 * D_) + col0 + 8 * c;
#pragma unroll
        for (int i = 0; i < 8; i++) u[i] = *(const u32x4*)(q + (size_t)i * 8 * (3 * D_));
    }
    if (b < 12) ((u32x4*)cnt)[b * 256 + tid] = (u32x4){0u, 0u, 0u, 0u};
    else if (b < 26) ((u32x4*)((char*)cs + 4 * 3 * D_ * 4))[(b - 12) * 256 + tid] = (u32x4){0u, 0u, 0u, 0u};
    const __amdgpu_buffer_rsrc_t rT = mkrsrc(xt, S_ * D_ * 2);
#pragma unroll
    for (int t = 0; t < 4; t++) {
        const int nt = 4 * w + t;
        st16(rT, (((row >> 5) * (D_ / 8) + 8 * nt + c) * 256 + (row & 31) * 8) * 2, v[t]);
        float f[8];
#pragma unroll
        for (int j = 0; j < 4; j++) { f[2 * j] = bflo(v[t][j]); f[2 * j + 1] = bfhi(v[t][j]); }
        float s = ((f[0] + f[1]) + (f[2] + f[3])) + ((f[4] + f[5]) + (f[6] + f[7]));
        s = sum_half_mirror(sum_xor2(sum_xor1(s)));
        const float m = s * (1.0f / 64.0f);
        float q = 0.f;
#pragma unroll
        for (int j = 0; j < 8; j++) q += (f[j] - m) * (f[j] - m);
        q = sum_half_mirror(sum_xor2(sum_xor1(q)));
        if (c == 0) __hip_atomic_store((unsigned long long*)(st + (size_t)row * NST + nt), __builtin_bit_cast(unsigned long long, (f32x2){m, q}), __ATOMIC_RELAXED, __HIP_MEMORY_SCOPE_AGENT);
    }
    if (has_piece) {
        float s[8];
#pragma unroll
        for (int j = 0; j < 8; j++) s[j] = 0.f;
#pragma unroll
        for (int i = 0; i < 8; i++)
#pragma unroll
            for (int j = 0; j < 4; j++) { s[2 * j] += bflo(u[i][j]); s[2 * j + 1] += bfhi(u[i][j]); }
#pragma unroll
        for (int j = 0; j < 8; j++) s[j] = sum_swap32(sum_swap16(sum_ror8(s[j])));
        if (r == 0) {
#pragma unroll
            for (int j = 0; j < 8; j++) red[w][8 * c + j] = s[j];
        }
        __syncthreads();
        if (tid < 64) cs[(col0 + tid) * 4 + kq] = (red[0][tid] + red[1][tid]) + (red[2][tid] + red[3][tid]);
    }
}

DI float phi(float t) { return (__builtin_fmaxf(t, 0.f) + __builtin_amdgcn_exp2f(__builtin_fminf(t * 1.44269504f, 0.f))) + 1e-6f; }

DI unsigned w_read_base(int lane, int f16, int krow0) {
    const int g = lane >> 4, q = (lane & 15) >> 2, p = lane & 3, swz = (((q >> 1) & 1) << 1) | ((g & 1) << 2);
    return (krow0 + 8 * g + q) * 128 + (((2 * f16 + (p >> 1)) ^ swz) << 4) + (p & 1) * 8;
}
DI unsigned w_dma_off(int lane, int kg, unsigned ld2, unsigned colbyte) {
    const int r = lane >> 3, s = lane & 7;
    return (8 * kg + r) * ld2 + colbyte + ((s ^ ((((r >> 1) & 1) << 1) | ((kg & 1) << 2))) << 4);
}

static constexpr size_t OFF_CNT = 0, OFF_FLG = 16384, OFF_CS = 16384 + 32768, OFF_ST = 196608, ST_BYTES = (size_t)S_ * NST * 8;
static constexpr size_t OFF_CSG = OFF_CS + 4 * 3 * D_ * 4;
static constexpr size_t XB = (size_t)S_ * D_ * 2, KVP_BYTES = (size_t)16 * 16 * 2112 * 4;
static constexpr size_t OFF_XT = OFF_ST + 2 * ST_BYTES, OFF_HT = OFF_XT + 2 * XB, OFF_OT = OFF_HT + (size_t)S_ * F_ * 2, OFF_KVP = OFF_OT + XB;
static constexpr size_t WS_NEED = OFF_KVP + 2 * KVP_BYTES;
static constexpr int C_KV = 0, C_O = 2048, C_X1 = 2048 + 32 * 16, C_H = 2048 + 64 * 16, C_X2 = 2048 + 96 * 16;

struct Params { const bf16_t *wqkv, *wo, *wg, *wu, *wd; bf16_t* out; char* ws; };

typedef const __attribute__((address_space(4))) Params* KArg;
DI KArg fresh_args() { KArg k = (KArg)__builtin_amdgcn_kernarg_segment_ptr(); asm volatile("" : "+s"(k)); return k; }
DI int fresh_tid() { int t = threadIdx.x; asm volatile("" : "+v"(t)); return t; }
DI int fresh_bid() { int b = blockIdx.x; asm volatile("" : "+s"(b)); return b; }

DI void signal(unsigned* flag, int tid) {
    asm volatile("s_waitcnt vmcnt(0)" ::: "memory");
    __syncthreads();
    if (tid == 0) __hip_atomic_store(flag, 1u, __ATOMIC_RELAXED, __HIP_MEMORY_SCOPE_AGENT);
}
DI void await(unsigned* c, int n, int w, int lane) {
    if (w == 0) {
        unsigned* q = c + (lane < n ? lane : 0);
        for (unsigned spins = 0; spins < (1u << 21); ++spins)
            if (__all(__hip_atomic_load(q, __ATOMIC_RELAXED, __HIP_MEMORY_SCOPE_AGENT) != 0u)) break;
    }
    __syncthreads();
}
DI u32x4 ld16(__amdgpu_buffer_rsrc_t r, unsigned off) { return __builtin_amdgcn_raw_buffer_load_b128(r, off, 0, 16); }
DI unsigned rotpos(unsigned u, unsigned rot, unsigned C) { return (u & ~(C - 1)) | ((u + rot) & (C - 1)); }

template <int L>
DI void phase_qkv(char* lds) {
    const KArg p = fresh_args();
    const int tid = fresh_tid(), lane = tid & 63;
    const int w = __builtin_amdgcn_readfirstlane(tid >> 6), mw = w >> 1, hd = w & 1;
    const int bid = fresh_bid(), xcd = bid & 7, idx = bid >> 3;
    const int pm = (xcd & 1) * 8 + (idx & 7), hp = (xcd >> 1) * 4 + (idx >> 3);
    unsigned* const cnt = (unsigned*)(p->ws + OFF_CNT);
    const bf16_t* xt = (const bf16_t*)(p->ws + OFF_XT);
    const f32x2* st = (const f32x2*)(p->ws + OFF_ST);
    const float* cs = (const float*)(p->ws + OFF_CS);
    unsigned vxd[1], vwd[3], vxr[1], vwr[2];
#pragma unroll
    for (int j = 0; j < 3; j++) { const int i = 3 * w + j; vwd[j] = w_dma_off(lane, i & 7, 3 * D_ * 2, ((i >> 3) * D_ + 64 * hp) * 2) - j * 1024; }
    const __amdgpu_buffer_rsrc_t rX = mkrsrc(xt, S_ * D_ * 2), rW = mkrsrc(p->wqkv, 3 * D_ * D_ * 2);
    const unsigned rot = ((idx & 7) + 2 * (idx >> 3)) & 7;
    const unsigned m0x = w * 2048, m0w = w * 3072;
    { const unsigned s0 = rotpos(0, rot, 8) * (64 * 3 * D_ * 2), s1 = rotpos(1, rot, 8) * (64 * 3 * D_ * 2); QKV_PROW(vwd, rW, m0w, s0, s1); }
    vxd[0] = (pm * 4 + (w >> 1)) * (D_ / 8 * 512) + (w & 1) * 2048 + lane * 16;
    const unsigned sx0 = rotpos(0, rot, 8) * 4096, sx1 = rotpos(1, rot, 8) * 4096;
    const int arow = pm * 128 + ((tid & 255) >> 1);
    const float* const csp = cs + (((tid < 192 ? tid : 0) >> 6) * D_ + 64 * hp + (tid & 63)) * 4;
    asm volatile("" :: "v"(vxd[0]), "s"(sx0), "s"(sx1), "v"(arow), "v"(csp));
    if (L == 1) {
        if (T_AWAIT && w >= 1 && w <= 3) {
            const int Ln = idx * 192 + (w - 1) * 64 + lane, row = 512 + Ln / 12, sg = Ln % 12;
            l2_touch(rW, row * (3 * D_ * 2) + (sg >> 2) * (D_ * 2) + (xcd >> 1) * 512 + (sg & 3) * 128, QKV_LDS + 1792);
        }
        await(cnt + C_X2 + pm * 16, 16, w, lane);
    }
    Aux4 ax;
    AUX4_LOAD(ax, st, arow, tid & 1, csp);
    QKV_PROX(vxd, rX, m0x, sx0, sx1);
    unsigned uu = 2, tp, sX = rotpos(2, rot, 8) * 4096, sW = rotpos(2, rot, 8) * (64 * 3 * D_ * 2), cntr;
    const __amdgpu_buffer_rsrc_t rP = mkrsrc(p->ws + OFF_KVP + L * KVP_BYTES + (size_t)hp * 16 * 2112 * 4, 16 * 2112 * 4);
    unsigned* const cword = cnt + C_KV + (L * 16 + hp) * 64;
    vxr[0] = (lane >> 4) * 512 + (lane & 15) * 16 + mw * 4096;
    vwr[0] = w_read_base(lane, 2 * hd, 0); vwr[1] = w_read_base(lane, 2 * hd + 1, 0);
    LAS f32x2* stt = (LAS f32x2*)(lds + QKV_LDS);
    LAS float* cst = (LAS float*)(lds + QKV_LDS + 1024);
    AUX4_WAIT(ax, 4);
    { const f32x2 s = aux_stats(ax); if (tid < 256 && (tid & 1) == 0) stt[tid >> 1] = s; if (tid < 192) cst[tid] = (ax.c[0] + ax.c[1]) + (ax.c[2] + ax.c[3]); }
    const __amdgpu_buffer_rsrc_t rO = mkrsrc(p->ws + OFF_OT, S_ * D_ * 2);
    asm volatile("" :: "s"(rO), "s"(rP), "s"(cword));
    f32x16 acc[3];
    PRIO_ON(w, 1);
    QKV_MAIN(acc, vxd, vwd, vxr, vwr, rX, rW, sX, sW, m0x, m0w, cntr, uu, rot, tp);
    PRIO_OFF(1);
    if (w >= 1 && w <= 2) {
        const int Ln = idx * 128 + (w - 1) * 64 + lane;
        l2_touch(mkrsrc(p->wo, D_ * D_ * 2), (Ln >> 2) * (D_ * 2) + (xcd >> 1) * 512 + (Ln & 3) * 128, QKV_LDS + 1792);
    }
#define QF(wi, xi) fr4(acc[(2 * (wi) + (xi)) >> 2], (2 * (wi) + (xi)) & 3)
    const int tl = mw * 32;
    const int i16 = lane & 15, g4 = lane >> 4;
    const int colh = 64 * hp + 32 * hd;
    {
        f32x4 mean[2], rstd[2]; float ck[2], cv[2];
#pragma unroll
        for (int tf = 0; tf < 2; tf++) {
            const LAS f32x4* sp = (const LAS f32x4*)(stt + tl + 16 * tf + 4 * g4);
            const f32x4 s0 = sp[0], s1 = sp[1];
            mean[tf] = (f32x4){s0[0], s0[2], s1[0], s1[2]}; rstd[tf] = (f32x4){s0[1], s0[3], s1[1], s1[3]};
            ck[tf] = cst[64 + 32 * hd + 16 * tf + i16]; cv[tf] = cst[128 + 32 * hd + 16 * tf + i16];
        }
        u32x4 pk[2], vv[2];
#pragma unroll
        for (int ff = 0; ff < 2; ff++)
#pragma unroll
            for (int tf = 0; tf < 2; tf++) {
                const f32x4 tk = (QF(2 + ff, tf) - mean[tf] * ck[ff]) * rstd[tf];
                const f32x4 tv = (QF(4 + ff, tf) - mean[tf] * cv[ff]) * rstd[tf];
                pk[ff][2 * tf] = cvtpk(phi(tk[0]), phi(tk[1])); pk[ff][2 * tf + 1] = cvtpk(phi(tk[2]), phi(tk[3]));
                vv[ff][2 * tf] = cvtpk(tv[0], tv[1]); vv[ff][2 * tf + 1] = cvtpk(tv[2], tv[3]);
            }
        const f32x4 z4 = {0.f, 0.f, 0.f, 0.f};
        const u32x4 ones = {0x3f803f80u, 0x3f803f80u, 0x3f803f80u, 0x3f803f80u};
        LAS f32x4* pw = (LAS f32x4*)(lds + QKV_STAGE) + w * 6 * 64 + lane;
#pragma unroll
        for (int fd = 0; fd < 2; fd++) {
#pragma unroll
            for (int fe = 0; fe < 2; fe++) pw[(2 * fd + fe) * 64] = mfma16(pk[fd], vv[fe], z4);
            pw[(4 + fd) * 64] = mfma16(pk[fd], ones, z4);
        }
    }
    __syncthreads();
    {
        const int h2 = tid >> 8, e = tid & 255;
        const LAS f32x4* q = (const LAS f32x4*)(lds + QKV_STAGE) + h2 * 384 + e;
        const f32x4 s = (q[0] + q[768]) + (q[2 * 768] + q[3 * 768]);
        __builtin_amdgcn_raw_buffer_store_b128(__builtin_bit_cast(u32x4, s), rP, (pm * 528 + tid) * 16, 0, 16);
        if (tid < 16) {
            const int h3 = tid >> 3, fd = (tid >> 2) & 1, g = tid & 3;
            const LAS f32x4* pz = (const LAS f32x4*)(lds + QKV_STAGE) + h3 * 384 + (4 + fd) * 64 + 16 * g;
            const f32x4 z = (pz[0] + pz[768]) + (pz[2 * 768] + pz[3 * 768]);
            __builtin_amdgcn_raw_buffer_store_b128(__builtin_bit_cast(u32x4, z), rP, (pm * 528 + 512 + tid) * 16, 0, 16);
        }
    }
    u32x4 pq[2];
    {
#pragma unroll
        for (int tf = 0; tf < 2; tf++) {
            const f32x2 s = stt[tl + 16 * tf + i16];
#pragma unroll
            for (int ff = 0; ff < 2; ff++) {
                const f32x4 c4 = *(const LAS f32x4*)(cst + 32 * hd + 16 * ff + 4 * g4);
                const f32x4 t = (QF(ff, tf) - s.x * c4) * s.y;
                pq[tf][2 * ff] = cvtpk(phi(t[0]), phi(t[1])); pq[tf][2 * ff + 1] = cvtpk(phi(t[2]), phi(t[3]));
            }
        }
    }
    asm volatile("" : "+v"(pq[0]), "+v"(pq[1]));
    signal(cword + pm, tid);
    await(cword, 16, w, lane);
    LAS u32x2* kvb = (LAS u32x2*)lds;
    LAS u32x2* zb = (LAS u32x2*)(lds + 4096);
    {
        u32x4 v[16], vz[16];
#pragma unroll
        for (int m = 0; m < 16; m++) v[m] = ld16(rP, (m * 528 + tid) * 16);
        if (w == 0) {
#pragma unroll
            for (int m = 0; m < 16; m++) vz[m] = ld16(rP, (m * 528 + 512 + (lane & 15)) * 16);
        }
        f32x4 s = {0.f, 0.f, 0.f, 0.f};
#pragma unroll
        for (int m = 0; m < 16; m++) s += __builtin_bit_cast(f32x4, v[m]);
        const int h2 = tid >> 8, fd = (tid >> 7) & 1, fe = (tid >> 6) & 1;
        kvb[((h2 * 2 + fe) * 64 + lane) * 2 + fd] = (u32x2){cvtpk(s[0], s[1]), cvtpk(s[2], s[3])};
        if (w == 0) {
            f32x4 z = {0.f, 0.f, 0.f, 0.f};
#pragma unroll
            for (int m = 0; m < 16; m++) z += __builtin_bit_cast(f32x4, vz[m]);
            const int h3 = (lane >> 3) & 1, fz = (lane >> 2) & 1, g = lane & 3;
            if (lane < 16) zb[(h3 * 4 + g) * 2 + fz] = (u32x2){cvtpk(z[0], z[1]), cvtpk(z[2], z[3])};
        }
    }
    __syncthreads();
    {
        const f32x4 z4 = {0.f, 0.f, 0.f, 0.f};
        const u32x4 za = *(const LAS u32x4*)(zb + (hd * 4 + g4) * 2);
        f32x4 den[2];
#pragma unroll
        for (int tf = 0; tf < 2; tf++) den[tf] = mfma16(za, pq[tf], z4);
        const float r0 = __builtin_amdgcn_rcpf(den[0][0] + 1e-6f), r1 = __builtin_amdgcn_rcpf(den[1][0] + 1e-6f);
        const int mblk = pm * 4 + mw;
#pragma unroll
        for (int fe = 0; fe < 2; fe++) {
            const u32x4 ka = *(const LAS u32x4*)(kvb + ((hd * 2 + fe) * 64 + lane) * 2);
            const f32x4 n0 = mfma16(ka, pq[0], z4) * r0, n1 = mfma16(ka, pq[1], z4) * r1;
            const auto s0 = __builtin_amdgcn_permlane16_swap(cvtpk(n0[0], n0[1]), cvtpk(n1[0], n1[1]), false, false);
            const auto s1 = __builtin_amdgcn_permlane16_swap(cvtpk(n0[2], n0[3]), cvtpk(n1[2], n1[3]), false, false);
            st16(rO, ((mblk * (D_ / 8) + ((colh + 16 * fe) >> 3) + (lane >> 5)) * 256 + (lane & 31) * 8) * 2, (u32x4){s0[0], s1[0], s0[1], s1[1]});
        }
    }
#undef QF
    signal(cnt + C_O + (L * 16 + pm) * 16 + hp, tid);
}

template <bool FINAL>
DI void proj_tail(const float (&xv)[2][8], const u32x4 (&rs)[2], __amdgpu_buffer_rsrc_t rO, f32x2* __restrict__ st, int mblk, int col, int lane, int tid, int mw, int wn, int row0, int stj, LAS f32x2* sx) {
    u32x4 o[2];
#pragma unroll
    for (int fe = 0; fe < 2; fe++)
#pragma unroll
        for (int j = 0; j < 4; j++) o[fe][j] = cvtpk(xv[fe][2 * j] + bflo(rs[fe][j]), xv[fe][2 * j + 1] + bfhi(rs[fe][j]));
    if constexpr (FINAL) {
        const int row = mblk * 32 + (lane & 31);
#pragma unroll
        for (int fe = 0; fe < 2; fe++) __builtin_amdgcn_raw_buffer_store_b128(o[fe], rO, (row * D_ + col + 16 * fe + 8 * (lane >> 5)) * 2, 0, FIN_AUX);
    } else {
#pragma unroll
        for (int fe = 0; fe < 2; fe++) st16(rO, ((mblk * (D_ / 8) + ((col + 16 * fe) >> 3) + (lane >> 5)) * 256 + (lane & 31) * 8) * 2, o[fe]);
        float f[16];
#pragma unroll
        for (int fe = 0; fe < 2; fe++)
#pragma unroll
            for (int j = 0; j < 4; j++) { f[8 * fe + 2 * j] = bflo(o[fe][j]); f[8 * fe + 2 * j + 1] = bfhi(o[fe][j]); }
        float s = 0.f;
#pragma unroll
        for (int j = 0; j < 16; j++) s += f[j];
        s = sum_swap32(s);
        const float m = s * (1.0f / 32.0f);
        float q = 0.f;
#pragma unroll
        for (int j = 0; j < 16; j++) q += (f[j] - m) * (f[j] - m);
        q = sum_swap32(q);
        if (lane < 32) sx[(mw * 32 + lane) * 2 + wn] = (f32x2){m, q};
        __syncthreads();
        if (tid < 128) {
            const f32x2 u = sx[tid * 2], v = sx[tid * 2 + 1];
            const float d = u.x - v.x;
            const f32x2 r = {0.5f * (u.x + v.x), u.y + v.y + 16.0f * d * d};
            __hip_atomic_store((unsigned long long*)(st + (size_t)(row0 + tid) * NST + stj), __builtin_bit_cast(unsigned long long, r), __ATOMIC_RELAXED, __HIP_MEMORY_SCOPE_AGENT);
        }
    }
}
DI void swap_pairs(const f32x4& a0, const f32x4& a1, float (&xv)[8]) {
#pragma unroll
    for (int r = 0; r < 4; r++) {
        const auto s = __builtin_amdgcn_permlane16_swap(__float_as_uint(a0[r]), __float_as_uint(a1[r]), false, false);
        xv[r] = __uint_as_float(s[0]); xv[4 + r] = __uint_as_float(s[1]);
    }
}

template <int L>
DI void phase_wo(char* lds) {
    const KArg p = fresh_args();
    const int tid = fresh_tid(), lane = tid & 63;
    const int w = __builtin_amdgcn_readfirstlane(tid >> 6), mw = w >> 1, wn = w & 1;
    const int bid = fresh_bid(), xcd = bid & 7, idx = bid >> 3;
    const int pm = (xcd & 1) * 8 + (idx & 7), pn = (xcd >> 1) * 4 + (idx >> 3);
    unsigned* const cnt = (unsigned*)(p->ws + OFF_CNT);
    unsigned vxd[1], vwd[1], vxr[1], vwr[2];
    vwd[0] = w_dma_off(lane, w, D_ * 2, 64 * pn * 2);
    const __amdgpu_buffer_rsrc_t rX = mkrsrc(p->ws + OFF_OT, S_ * D_ * 2), rW = mkrsrc(p->wo, D_ * D_ * 2);
    const unsigned rot = ((idx & 7) + 2 * (idx >> 3)) & 7;
    const unsigned m0x = w * 2048, m0w = w * 1024;
    { const unsigned s0 = rotpos(0, rot, 8) * (64 * D_ * 2), s1 = rotpos(1, rot, 8) * (64 * D_ * 2); WO_PROW(vwd, rW, m0w, s0, s1); }
    vxd[0] = (pm * 4 + (w >> 1)) * (D_ / 8 * 512) + (w & 1) * 2048 + lane * 16;
    const unsigned sx0 = rotpos(0, rot, 8) * 4096, sx1 = rotpos(1, rot, 8) * 4096;
    asm volatile("" :: "v"(vxd[0]), "s"(sx0), "s"(sx1));
    await(cnt + C_O + (L * 16 + pm) * 16, 16, w, lane);
    WO_PROX(vxd, rX, m0x, sx0, sx1);
    unsigned uu = 2, tp, sX = rotpos(2, rot, 8) * 4096, sW = rotpos(2, rot, 8) * (64 * D_ * 2), cntr;
    vxr[0] = (lane >> 4) * 512 + (lane & 15) * 16 + mw * 4096;
    vwr[0] = w_read_base(lane, 2 * wn, 0); vwr[1] = w_read_base(lane, 2 * wn + 1, 0);
    const int mblk = pm * 4 + mw, col = 64 * pn + 32 * wn;
    const __amdgpu_buffer_rsrc_t rR = mkrsrc(p->ws + OFF_XT, S_ * D_ * 2), rO = mkrsrc(p->ws + OFF_XT + XB, S_ * D_ * 2);
    u32x4 rs[2];
#pragma unroll
    for (int fe = 0; fe < 2; fe++) rs[fe] = ld16(rR, ((mblk * (D_ / 8) + ((col + 16 * fe) >> 3) + (lane >> 5)) * 256 + (lane & 31) * 8) * 2);
    f32x2* const st = (f32x2*)(p->ws + OFF_ST + ST_BYTES);
    asm volatile("" :: "s"(rO), "s"(st));
    f32x16 acc[1];
    PRIO_ON(w, 2);
    WO_MAIN(acc, vxd, vwd, vxr, vwr, rX, rW, sX, sW, m0x, m0w, cntr, uu, rot, tp);
    PRIO_OFF(2);
    float xv[2][8];
#pragma unroll
    for (int fe = 0; fe < 2; fe++) swap_pairs(fr4(acc[0], 2 * fe), fr4(acc[0], 2 * fe + 1), xv[fe]);
    proj_tail<false>(xv, rs, rO, st, mblk, col, lane, tid, mw, wn, pm * 128, pn, (LAS f32x2*)(lds + WO_LDS));
    signal(cnt + C_X1 + (L * 16 + pm) * 16 + pn, tid);
}

template <int L>
DI void phase_gu(char* lds) {
    const KArg p = fresh_args();
    constexpr bool FIRST = L == 0;
    const int tid = fresh_tid(), lane = tid & 63;
    const int w = __builtin_amdgcn_readfirstlane(tid >> 6), wm = w >> 2, wn = w & 3;
    const int bid = fresh_bid(), xcd = bid & 7, idx = bid >> 3;
    if (idx >= 28) {
        const unsigned long long t0 = __builtin_amdgcn_s_memrealtime();
        if constexpr (FIRST) {
            unsigned long long* const cs = (unsigned long long*)(p->ws + OFF_CSG);
            const int r = lane >> 2, c = lane & 3, strip = (xcd * 4 + (idx - 28)) * 7 + w;
            if (w < 7) {
                const bf16_t* q = (strip < 112 ? p->wg + 32 * strip : p->wu + 32 * (strip - 112)) + (size_t)r * F_ + 8 * c;
                float sm[8];
#pragma unroll
                for (int j = 0; j < 8; j++) sm[j] = 0.f;
#pragma unroll 1
                for (int h = 0; h < 2; h++) {
                    u32x4 v[32];
#pragma unroll
                    for (int i = 0; i < 32; i++) v[i] = *(const u32x4*)(q + (size_t)(h * 32 + i) * 16 * F_);
#pragma unroll
                    for (int i = 0; i < 32; i++)
#pragma unroll
                        for (int j = 0; j < 4; j++) { sm[2 * j] += bflo(v[i][j]); sm[2 * j + 1] += bfhi(v[i][j]); }
                }
#pragma unroll
                for (int j = 0; j < 8; j++) sm[j] = sum_swap32(sum_swap16(sum_ror8(sum_ror4(sm[j]))));
                if (r == 0) {
#pragma unroll
                    for (int j = 0; j < 8; j++) __hip_atomic_store(cs + 32 * strip + 8 * c + j, (1ull << 32) | __float_as_uint(sm[j]), __ATOMIC_RELAXED, __HIP_MEMORY_SCOPE_AGENT);
                }
            }
        }
        while (__builtin_amdgcn_s_memrealtime() - t0 < (FIRST ? TOUCH_T1 : TOUCH_T2)) __builtin_amdgcn_s_sleep(32);
        const __amdgpu_buffer_rsrc_t rT = mkrsrc(p->wd, F_ * D_ * 2);
        for (int i = 0; i < 7; i++) {
            const int Ln = (((idx - 28) * 8 + w) * 7 + i) * 64 + lane;
            l2_touch(rT, ((xcd & 1) * (F_ / 2) + (Ln >> 3)) * (D_ * 2) + (xcd >> 2) * 1024 + (Ln & 7) * 128, 4096);
        }
        asm volatile("s_waitcnt vmcnt(0)" ::: "memory");
        __syncthreads();
        return;
    }
    const int pm = (xcd & 1) * 4 + (idx & 3), pn = (xcd >> 1) * 7 + (idx >> 2);
    unsigned* const cnt = (unsigned*)(p->ws + OFF_CNT);
    unsigned long long* const csgr = (unsigned long long*)(p->ws + OFF_CSG);
    unsigned vxd[1], vwd[2], vxr[1], vwr[2];
    const int sub = w & 3, kh = w >> 2;
#pragma unroll
    for (int j = 0; j < 2; j++) vwd[j] = w_dma_off(lane, 2 * kh + j, F_ * 2, (pn * 128 + (sub & 1) * 64) * 2) - j * 1024;
    const __amdgpu_buffer_rsrc_t rX = mkrsrc(p->ws + OFF_XT + XB, S_ * D_ * 2), rW = mkrsrc(sub < 2 ? p->wg : p->wu, D_ * F_ * 2);
    const unsigned rot = ((idx >> 2) + 2 * (idx & 3)) & 7;
    const unsigned m0x = w * 2048, m0w = sub * 4096 + kh * 2048;
    { const unsigned s0 = rotpos(0, rot, 8) * (32 * F_ * 2), s1 = rotpos(1, rot, 8) * (32 * F_ * 2); GU_PROW(vwd, rW, m0w, s0, s1); }
    vxd[0] = (pm * 8 + w) * (D_ / 8 * 512) + lane * 16;
    const unsigned sx0 = rotpos(0, rot, 8) * 2048, sx1 = rotpos(1, rot, 8) * 2048;
    const f32x2* const stp = (const f32x2*)(p->ws + OFF_ST + ST_BYTES);
    const float* const csp = (const float*)(csgr + ((tid & 128) ? F_ : 0) + pn * 128 + (tid & 127));
    asm volatile("" :: "v"(vxd[0]), "s"(sx0), "s"(sx1), "s"(stp), "v"(csp));
    await(cnt + C_X1 + (L * 16 + 2 * pm) * 16, 32, w, lane);
    Aux ax;
    AUX_LOAD(ax, stp, pm * 256 + (tid >> 1), tid & 1, csp);
    GU_PROX(vxd, rX, m0x, sx0, sx1);
    unsigned uu = 2, tp, sX = rotpos(2, rot, 8) * 2048, sW = rotpos(2, rot, 8) * (32 * F_ * 2), cntr;
    vxr[0] = (lane >> 4) * 512 + (lane & 15) * 16 + wm * 4 * 2048;
    vwr[0] = w_read_base(lane, 2 * (wn & 1), 0) + (wn >> 1) * 4096; vwr[1] = w_read_base(lane, 2 * (wn & 1) + 1, 0) + (wn >> 1) * 4096;
    LAS f32x2* stt = (LAS f32x2*)(lds + GU_LDS);
    LAS float* cst = (LAS float*)(lds + GU_LDS + 2048);
    AUX_WAIT(ax, 4);
    { const f32x2 s = aux_stats(ax); if ((tid & 1) == 0) stt[tid >> 1] = s; if (!FIRST && tid < 256) cst[tid] = ax.c; }
    const __amdgpu_buffer_rsrc_t rH = mkrsrc(p->ws + OFF_HT, S_ * F_ * 2);
    asm volatile("" :: "s"(rH));
    f32x16 acc[8];
    PRIO_ON(w, 4);
    GU_MAIN(acc, vxd, vwd, vxr, vwr, rX, rW, sX, sW, m0x, m0w, cntr, uu, rot, tp);
    PRIO_OFF(4);
    if constexpr (FIRST) {
        float cv = 0.f;
        for (unsigned spins = 0; spins < (1u << 21); ++spins) {
            const unsigned long long g = __hip_atomic_load(csgr + (tid & 128 ? F_ : 0) + pn * 128 + (tid & 127), __ATOMIC_RELAXED, __HIP_MEMORY_SCOPE_AGENT);
            cv = __uint_as_float((unsigned)g);
            if (__all((g >> 32) != 0ull)) break;
        }
        if (tid < 256) cst[tid] = cv;
        __syncthreads();
    }
    const int n0 = pn * 128 + wn * 32;
    f32x4 cg4[2], cu4[2];
#pragma unroll
    for (int f = 0; f < 2; f++) { cg4[f] = *(const LAS f32x4*)(cst + wn * 32 + 16 * f + 4 * (lane >> 4)); cu4[f] = *(const LAS f32x4*)(cst + 128 + wn * 32 + 16 * f + 4 * (lane >> 4)); }
#pragma unroll
    for (int b = 0; b < 4; b++) {
        f32x2 s2[2];
#pragma unroll
        for (int mh = 0; mh < 2; mh++) s2[mh] = stt[wm * 128 + b * 32 + mh * 16 + (lane & 15)];
#pragma unroll
        for (int f = 0; f < 2; f++) {
            unsigned o[2][2];
#pragma unroll
            for (int mh = 0; mh < 2; mh++) {
                const int xi = 2 * b + mh;
                const float r_ = s2[mh].y, nm = -s2[mh].x, c1 = -1.44269504f * r_, ic2 = __builtin_amdgcn_rcpf(r_ * r_);
                const int fg = f * 8 + xi, fu = (2 + f) * 8 + xi;
                const f32x4 ag = fr4(acc[fg >> 2], fg & 3), au = fr4(acc[fu >> 2], fu & 3);
#pragma unroll
                for (int i = 0; i < 2; i++) {
                    const f32x2 g = (f32x2){ag[2 * i], ag[2 * i + 1]} + nm * (f32x2){cg4[f][2 * i], cg4[f][2 * i + 1]};
                    const f32x2 u = (f32x2){au[2 * i], au[2 * i + 1]} + nm * (f32x2){cu4[f][2 * i], cu4[f][2 * i + 1]};
                    const f32x2 tt = g * c1;
                    f32x2 ex = {__builtin_amdgcn_exp2f(tt.x), __builtin_amdgcn_exp2f(tt.y)};
                    ex = ex * ic2 + ic2;
                    const f32x2 rc = {__builtin_amdgcn_rcpf(ex.x), __builtin_amdgcn_rcpf(ex.y)};
                    const f32x2 hv = (g * u) * rc;
                    o[mh][i] = cvtpk(hv.x, hv.y);
                }
            }
            const auto s0 = __builtin_amdgcn_permlane16_swap(o[0][0], o[1][0], false, false);
            const auto s1 = __builtin_amdgcn_permlane16_swap(o[0][1], o[1][1], false, false);
            const int mblk = pm * 8 + wm * 4 + b;
            st16(rH, ((mblk * (F_ / 8) + ((n0 + 16 * f) >> 3) + (lane >> 5)) * 256 + (lane & 31) * 8) * 2, (u32x4){s0[0], s1[0], s0[1], s1[1]});
        }
    }
    signal(cnt + C_H + (L * 16 + pm * 2 + (pn >= 14)) * 16 + pn % 14, tid);
}

template <int L>
DI void phase_down(char* lds) {
    const KArg p = fresh_args();
    constexpr bool FINAL = L == 1;
    const int tid = fresh_tid(), lane = tid & 63;
    const int w = __builtin_amdgcn_readfirstlane(tid >> 6), mw = w >> 1, wn = w & 1;
    const int bid = fresh_bid(), xcd = bid & 7, idx = bid >> 3;
    const int kh = xcd & 1, pm = ((xcd >> 1) & 1) * 8 + (idx & 7), pn = (xcd >> 2) * 4 + (idx >> 3);
    unsigned* const cnt = (unsigned*)(p->ws + OFF_CNT);
    unsigned vxd[1], vwd[2], vxr[1], vwr[4];
#pragma unroll
    for (int j = 0; j < 2; j++) vwd[j] = w_dma_off(lane, 2 * (w & 3) + j, D_ * 2, (128 * pn + 64 * (w >> 2)) * 2) + kh * (F_ / 2 * D_ * 2) - j * 1024;
    const __amdgpu_buffer_rsrc_t rX = mkrsrc(p->ws + OFF_HT, S_ * F_ * 2), rW = mkrsrc(p->wd, F_ * D_ * 2);
    const unsigned rot = ((idx >> 3) + (idx & 7)) & 3;
    const unsigned m0x = w * 2048, m0w = w * 2048;
    { const unsigned s0 = rotpos(0, rot, 4) * (64 * D_ * 2), s1 = rotpos(1, rot, 4) * (64 * D_ * 2); DN_PROW(vwd, rW, m0w, s0, s1); }
    vxd[0] = (pm * 4 + (w >> 1)) * (F_ / 8 * 512) + kh * (F_ / 16 * 512) + (w & 1) * 2048 + lane * 16;
    const unsigned sx0 = rotpos(0, rot, 4) * 4096, sx1 = rotpos(1, rot, 4) * 4096;
    asm volatile("" :: "v"(vxd[0]), "s"(sx0), "s"(sx1));
    await(cnt + C_H + (L * 16 + (pm >> 1) * 2 + kh) * 16, 14, w, lane);
    DN_PROX(vxd, rX, m0x, sx0, sx1);
    unsigned uu = 2, tp, sX = rotpos(2, rot, 4) * 4096, sW = rotpos(2, rot, 4) * (64 * D_ * 2), cntr;
    vxr[0] = (lane >> 4) * 512 + (lane & 15) * 16 + mw * 4096;
#pragma unroll
    for (int wi = 0; wi < 4; wi++) vwr[wi] = w_read_base(lane, wi ^ (2 * kh), 0) + wn * 8192;
    const int mblk = pm * 4 + mw, col = 128 * pn + 64 * wn + 32 * kh;
    const __amdgpu_buffer_rsrc_t rR = mkrsrc(p->ws + OFF_XT + XB, S_ * D_ * 2);
    const __amdgpu_buffer_rsrc_t rO = mkrsrc(FINAL ? (char*)p->out : p->ws + OFF_XT, S_ * D_ * 2);
    u32x4 rs[2];
#pragma unroll
    for (int fe = 0; fe < 2; fe++) rs[fe] = ld16(rR, ((mblk * (D_ / 8) + ((col + 16 * fe) >> 3) + (lane >> 5)) * 256 + (lane & 31) * 8) * 2);
    f32x2* const st = (f32x2*)(p->ws + OFF_ST);
    const __amdgpu_buffer_rsrc_t rE = mkrsrc(p->ws + OFF_OT + (size_t)pm * (128 * D_ * 2) + pn * 32768, 32768);
    unsigned* const flags = (unsigned*)(p->ws + OFF_FLG) + L * 256 * 16;
    unsigned* const fme = flags + ((pm * 8 + pn) * 2 + kh) * 16;
    unsigned* const fot = flags + ((pm * 8 + pn) * 2 + (kh ^ 1)) * 16;
    asm volatile("" :: "s"(rO), "s"(st), "s"(rE), "s"(fme), "s"(fot));
    f32x16 acc[2];
    PRIO_ON(w, 8);
    DN_MAIN(acc, vxd, vwd, vxr, vwr, rX, rW, sX, sW, m0x, m0w, cntr, uu, rot, tp);
    PRIO_OFF(8);
#pragma unroll
    for (int j = 0; j < 2; j++) {
        const f32x4 a = fr4(acc[1], 2 * j), b = fr4(acc[1], 2 * j + 1);
        __builtin_amdgcn_raw_buffer_store_b128((u32x4){cvtpk(a[0], a[1]), cvtpk(a[2], a[3]), cvtpk(b[0], b[1]), cvtpk(b[2], b[3])}, rE, kh * 16384 + ((w * 2 + j) * 64 + lane) * 16, 0, 16);
    }
    signal(fme, tid);
    await(fot, 1, w, lane);
    float xv[2][8];
#pragma unroll
    for (int fe = 0; fe < 2; fe++) {
        const u32x4 r = ld16(rE, (kh ^ 1) * 16384 + ((w * 2 + fe) * 64 + lane) * 16);
        const f32x4 a = fr4(acc[0], 2 * fe) + (f32x4){bflo(r[0]), bfhi(r[0]), bflo(r[1]), bfhi(r[1])}, b = fr4(acc[0], 2 * fe + 1) + (f32x4){bflo(r[2]), bfhi(r[2]), bflo(r[3]), bfhi(r[3])};
        swap_pairs(a, b, xv[fe]);
    }
    if (T_TAIL && !FINAL && w >= 5) {
        const int Ln = idx * 192 + (w - 5) * 64 + lane, row = Ln / 12, sg = Ln % 12;
        l2_touch(mkrsrc(p->wqkv, 3 * D_ * D_ * 2), row * (3 * D_ * 2) + (sg >> 2) * (D_ * 2) + (xcd >> 1) * 512 + (sg & 3) * 128, DN_LDS + 2048);
    }
    proj_tail<FINAL>(xv, rs, rO, st, mblk, col, lane, tid, mw, wn, pm * 128, 2 * pn + kh, (LAS f32x2*)(lds + DN_LDS));
    if (!FINAL) signal(cnt + C_X2 + pm * 16 + 2 * pn + kh, tid);
}

template <int FIRST, int LAST>
__global__ __launch_bounds__(512, 2) void k_mega(const Params p) {
    extern __shared__ __attribute__((aligned(16))) char lds[];
    if constexpr (FIRST <= 0 && 0 <= LAST) phase_qkv<0>(lds);
    if constexpr (FIRST <= 1 && 1 <= LAST) phase_wo<0>(lds);
    if constexpr (FIRST <= 2 && 2 <= LAST) phase_gu<0>(lds);
    if constexpr (FIRST <= 3 && 3 <= LAST) phase_down<0>(lds);
    if constexpr (FIRST <= 4 && 4 <= LAST) phase_qkv<1>(lds);
    if constexpr (FIRST <= 5 && 5 <= LAST) phase_wo<1>(lds);
    if constexpr (FIRST <= 6 && 6 <= LAST) phase_gu<1>(lds);
    if constexpr (FIRST <= 7 && 7 <= LAST) phase_down<1>(lds);
}
#define SPLIT_MASK 0
template <int FIRST> struct Part {
    static constexpr int last() { int l = FIRST; while (l < 7 && !((SPLIT_MASK >> l) & 1)) l++; return l; }
    static void launch(const Params& p, int lds, hipStream_t stream) {
        constexpr int L = last();
        (void)hipFuncSetAttribute((const void*)k_mega<FIRST, L>, hipFuncAttributeMaxDynamicSharedMemorySize, lds);
        k_mega<FIRST, L><<<256, 512, lds, stream>>>(p);
        if constexpr (L < 7) Part<L + 1>::launch(p, lds, stream);
    }
};

extern "C" void kernel_launch(void* const* d_in, const int* in_sizes, int n_in, void* d_out, int out_size, void* d_ws, size_t ws_size, hipStream_t stream) {
#define WS_POS 1
    char* ws = (char*)d_ws + (WS_POS == 0 ? 0 : WS_POS == 1 ? ((ws_size - WS_NEED) & ~(size_t)4095) : (ws_size / 2 & ~(size_t)4095));
    Params p;
    p.wqkv = (const bf16_t*)d_in[1]; p.wo = (const bf16_t*)d_in[2]; p.wg = (const bf16_t*)d_in[4]; p.wu = (const bf16_t*)d_in[5]; p.wd = (const bf16_t*)d_in[6];
    p.out = (bf16_t*)d_out; p.ws = ws;
    constexpr int LDS = GU_LDS + 3328;
    static_assert(LDS >= QKV_LDS + 2048 && LDS >= WO_LDS + 2048 && LDS >= DN_LDS + 2304, "dynamic LDS covers every phase");
    k_prep<<<256, 256, 0, stream>>>((const bf16_t*)d_in[0], p.wqkv, (bf16_t*)(ws + OFF_XT), (f32x2*)(ws + OFF_ST), (float*)(ws + OFF_CS), (unsigned*)(ws + OFF_CNT));
    Part<0>::launch(p, LDS, stream);
}
```
